```python
import torch

def _softmax(x, axis=-1):
    return torch.softmax(x, dim=axis)

def _gelu(x):
    return torch.nn.functional.gelu(x, approximate='tanh')

def _silu(x):
    return torch.nn.functional.silu(x)

def _sigmoid(x):
    return torch.sigmoid(x)

def _layernorm(x, axis=-1, eps=1e-05):
    m = x.mean(dim=axis, keepdim=True)
    v = x.var(dim=axis, keepdim=True, unbiased=False)
    return (x - m) / torch.sqrt(v + eps)

def _scatter_add(tgt, idx, src, axis=0):
    return tgt.clone().index_add_(axis, idx, src)

def _segment_sum(src, seg, n):
    out = torch.zeros((n,) + src.shape[1:], dtype=src.dtype)
    return out.index_add_(0, seg, src)

def _pad(x, pw):
    flat = tuple((v for pair in reversed(pw) for v in pair))
    return torch.nn.functional.pad(x, flat)
kernel_name = 'parallel_sum__spectral_fnet__mixer_gmlp__prompt'

def setup_inputs():

    _w = lambda a: (lambda t: t.to(torch.bfloat16) if t.is_floating_point() else t)(torch.as_tensor(a))

    def _A_setup():
        B, S, D = (1, 2048, 512)
        ns = torch.arange(S)[:, None] * torch.arange(S)[None, :]
        nd = torch.arange(D)[:, None] * torch.arange(D)[None, :]
        Ws_re = torch.cos(-2.0 * torch.pi * ns / S).to(float)
        Ws_im = torch.sin(-2.0 * torch.pi * ns / S).to(float)
        Wd_re = torch.cos(-2.0 * torch.pi * nd / D).to(float)
        Wd_im = torch.sin(-2.0 * torch.pi * nd / D).to(float)
        return {'x': _w((lambda *s: torch.randn(*s, dtype=torch.bfloat16) * 0.05)(B, S, D)), 'dft_seq_re': _w(Ws_re), 'dft_seq_im': _w(Ws_im), 'dft_hid_re': _w(Wd_re), 'dft_hid_im': _w(Wd_im)}
    _A_in = _A_setup()

    def _B_setup():
        B, S, D, F = (1, 2048, 512, 1792)
        Fh = F // 2
        return {'x': _w((lambda *s: torch.randn(*s, dtype=torch.bfloat16) * 0.05)(B, S, D)), 'w_in': _w((lambda *s: torch.randn(*s, dtype=torch.bfloat16) * 0.8)(D, 2 * Fh)), 'w_sp': _w(0.01 * (lambda *s: torch.randn(*s, dtype=torch.bfloat16) * 0.8)(S, S)), 'b_sp': _w(1.0 + 0.01 * (lambda *s: torch.randn(*s, dtype=torch.bfloat16) * 0.8)(S)), 'w_out': _w((lambda *s: torch.randn(*s, dtype=torch.bfloat16) * 0.8)(Fh, D))}
    _B_in = _B_setup()
    return {'A_x': _A_in['x'], 'A_dft_seq_re': _A_in['dft_seq_re'], 'A_dft_seq_im': _A_in['dft_seq_im'], 'A_dft_hid_re': _A_in['dft_hid_re'], 'A_dft_hid_im': _A_in['dft_hid_im'], 'B_x': _B_in['x'], 'B_w_in': _B_in['w_in'], 'B_w_sp': _B_in['w_sp'], 'B_b_sp': _B_in['b_sp'], 'B_w_out': _B_in['w_out']}

def reference(A_x, A_dft_seq_re, A_dft_seq_im, A_dft_hid_re, A_dft_hid_im, B_x, B_w_in, B_w_sp, B_b_sp, B_w_out):

    def _A_ref(x, dft_seq_re, dft_seq_im, dft_hid_re, dft_hid_im):
        a = torch.einsum('bsd,ed->bse', x, dft_hid_re)
        b = torch.einsum('bsd,ed->bse', x, dft_hid_im)
        y = torch.einsum('ts,bse->bte', dft_seq_re, a) - torch.einsum('ts,bse->bte', dft_seq_im, b)
        return y

    def _B_ref(x, w_in, w_sp, b_sp, w_out):
        B, S, D = x.shape
        F = w_in.shape[-1]
        Fh = F // 2
        z = _gelu(torch.einsum('bsd,df->bsf', x, w_in))
        z1 = z[..., :Fh].clone()
        z2 = _layernorm(z[..., Fh:], axis=-1)
        g = torch.einsum('ts,bsf->btf', w_sp, z2) + b_sp[None, :, None]
        return torch.einsum('bsf,fd->bsd', z1 * g, w_out)
    x = A_x
    xn = _layernorm(x, axis=-1)
    return x + _A_ref(xn, A_dft_seq_re, A_dft_seq_im, A_dft_hid_re, A_dft_hid_im) + _B_ref(xn, B_w_in, B_w_sp, B_b_sp, B_w_out)
```

```cpp
#include <hip/hip_runtime.h>
#include <hip/hip_bf16.h>
#include <stdint.h>

typedef unsigned short u16;
typedef __bf16 bf16x8 __attribute__((ext_vector_type(8)));
typedef float f32x4 __attribute__((ext_vector_type(4)));
typedef unsigned u32x4 __attribute__((ext_vector_type(4)));
typedef unsigned u32x2 __attribute__((ext_vector_type(2)));
#define LAS __attribute__((address_space(3)))
typedef __amdgpu_buffer_rsrc_t rsrc_t;

constexpr int S_ = 2048, D_ = 512, F_ = 1792, FH = 896;

__device__ __forceinline__ float bf2f(unsigned v) { return __uint_as_float(v << 16); }
__device__ __forceinline__ float bflo(unsigned w) { return __uint_as_float(w << 16); }
__device__ __forceinline__ float bfhi(unsigned w) { return __uint_as_float(w & 0xffff0000u); }
__device__ __forceinline__ unsigned pk_bf16(float lo, float hi) {
  typedef __bf16 bf16x2 __attribute__((ext_vector_type(2)));
  typedef float f32x2 __attribute__((ext_vector_type(2)));
  f32x2 v = {lo, hi};
  bf16x2 r = __builtin_convertvector(v, bf16x2);
  return __builtin_bit_cast(unsigned, r);
}
__device__ __forceinline__ float gelu_tanh(float x) {
  const float c1 = -2.0f * 0.7978845608028654f * 1.4426950408889634f;
  const float c3 = c1 * 0.044715f;
  float x2 = x * x;
  float t = x * __builtin_fmaf(x2, c3, c1);
  float e = __builtin_amdgcn_exp2f(t);
  return x * __builtin_amdgcn_rcpf(1.0f + e);
}
__device__ __forceinline__ rsrc_t mk_rsrc(const void* p, unsigned bytes) {
  return __builtin_amdgcn_make_buffer_rsrc((void*)p, (short)0, (int)bytes, 0x00020000);
}
#define WAIT_V(n) asm volatile("s_waitcnt vmcnt(%0)" ::"n"(n) : "memory")
#define WAIT_L(n) asm volatile("s_waitcnt lgkmcnt(%0)" ::"n"(n) : "memory")
#define BAR() __builtin_amdgcn_s_barrier()
#define SCHED() __builtin_amdgcn_sched_barrier(0)

template <int BI, int BJ, int NWI, int NWJ, int NS, int NK>
struct Gemm {
  static constexpr int NW = NWI * NWJ, WI = BI / NWI, WJ = BJ / NWJ, MT = WI / 16, NT = WJ / 16;
  static constexpr int SB = (BI + BJ) * 128;
  static constexpr int LP = BI / 8 / NW, LQ = BJ / 8 / NW, L = LP + LQ;
  static constexpr int LDS_BYTES = NS * SB;
  static_assert(BI % (8 * NW) == 0 && BJ % (8 * NW) == 0 && NW >= 4 && (MT % 2) == 0, "shape");
  static_assert(NK >= NS, "nk");

  static __device__ __forceinline__ void stage(LAS char* lds, int soff, rsrc_t rP, int pitchP, unsigned voffP, rsrc_t rQ,
                                               int pitchQ, unsigned voffQ, int kk, int w) {
#pragma unroll
    for (int t = 0; t < LP; ++t)
      __builtin_amdgcn_raw_ptr_buffer_load_lds(rP, (LAS void*)(lds + soff + (t * NW + w) * 1024), 16, voffP,
                                               kk * 128 + t * NW * 8 * pitchP, 0, 0);
#pragma unroll
    for (int t = 0; t < LQ; ++t)
      __builtin_amdgcn_raw_ptr_buffer_load_lds(rQ, (LAS void*)(lds + soff + BI * 128 + (t * NW + w) * 1024), 16, voffQ,
                                               kk * 128 + t * NW * 8 * pitchQ, 0, 0);
  }
  static __device__ __forceinline__ void rd(LAS char* lds, int aP, int aQ, bf16x8 (&p)[MT], bf16x8 (&q)[NT]) {
#pragma unroll
    for (int m = 0; m < MT; ++m) p[m] = *(const LAS bf16x8*)(lds + aP + m * 2048);
#pragma unroll
    for (int n = 0; n < NT; ++n) q[n] = *(const LAS bf16x8*)(lds + aQ + n * 2048);
  }
  static __device__ __forceinline__ void mma(f32x4 (&acc)[MT][NT], const bf16x8 (&p)[MT], const bf16x8 (&q)[NT]) {
#pragma unroll
    for (int m = 0; m < MT; ++m)
#pragma unroll
      for (int n = 0; n < NT; ++n) acc[m][n] = __builtin_amdgcn_mfma_f32_16x16x32_bf16(p[m], q[n], acc[m][n], 0, 0, 0);
  }

  static __device__ __forceinline__ void run(LAS char* lds, const void* Pg, int pitchP, const void* Qg, int pitchQ,
                                             f32x4 (&acc)[MT][NT], int w, int lane) {
    const rsrc_t rP = mk_rsrc(Pg, (unsigned)(BI * pitchP)), rQ = mk_rsrc(Qg, (unsigned)(BJ * pitchQ));
    const int rho = (8 * w + (lane >> 3));
    const int fsw = (rho >> 1) & 7;
    const int r32 = rho & 31;
    const int prow = (rho & ~31) + 8 * ((r32 >> 2) & 3) + 4 * (r32 >> 4) + (r32 & 3);
    const unsigned voffP = (unsigned)(prow * pitchP + (((lane & 7) ^ fsw) << 4));
    const unsigned voffQ = (unsigned)(rho * pitchQ + (((lane & 7) ^ fsw) << 4));
    const int wi = w / NWJ, wj = w % NWJ;
    const int fr = lane & 15, fq = lane >> 4;
    const int aP0 = (wi * WI + fr) * 128 + ((fq ^ (fr >> 1)) << 4);
    const int aQ0 = BI * 128 + (wj * WJ + fr) * 128 + ((fq ^ (fr >> 1)) << 4);
#pragma unroll
    for (int m = 0; m < MT; ++m)
#pragma unroll
      for (int n = 0; n < NT; ++n) acc[m][n] = (f32x4){0.f, 0.f, 0.f, 0.f};
    bf16x8 pa[MT], qa[NT], pb[MT], qb[NT];
#pragma unroll
    for (int s = 0; s < NS; ++s) stage(lds, s * SB, rP, pitchP, voffP, rQ, pitchQ, voffQ, s, w);
    WAIT_V((NS - 1) * L);
    BAR();
    asm volatile("" ::: "memory");
    rd(lds, aP0, aQ0, pa, qa);
    int soff = 0;
    for (int kk = 0; kk < NK - NS; ++kk) {
      rd(lds, soff + (aP0 ^ 64), soff + (aQ0 ^ 64), pb, qb);
      SCHED();
      mma(acc, pa, qa);
      SCHED();
      asm volatile("s_waitcnt vmcnt(%0) lgkmcnt(0)" ::"n"((NS - 2) * L) : "memory");
      BAR();
      asm volatile("" ::: "memory");
      stage(lds, soff, rP, pitchP, voffP, rQ, pitchQ, voffQ, kk + NS, w);
      soff += SB;
      if (soff == NS * SB) soff = 0;
      rd(lds, soff + aP0, soff + aQ0, pa, qa);
      SCHED();
      mma(acc, pb, qb);
      SCHED();
    }
#pragma unroll
    for (int u = 0; u < NS; ++u) {
      rd(lds, soff + (aP0 ^ 64), soff + (aQ0 ^ 64), pb, qb);
      SCHED();
      mma(acc, pa, qa);
      SCHED();
      if (u < NS - 1) {
        asm volatile("s_waitcnt vmcnt(%0) lgkmcnt(0)" ::"n"((NS - 2 - u) * L) : "memory");
        BAR();
        asm volatile("" ::: "memory");
        soff += SB;
        if (soff == NS * SB) soff = 0;
        rd(lds, soff + aP0, soff + aQ0, pa, qa);
        SCHED();
      }
      mma(acc, pb, qb);
      SCHED();
    }
  }
};

__global__ __launch_bounds__(256) void k0_prep(const u16* __restrict__ x, const u16* __restrict__ w_in,
                                               const u16* __restrict__ w_out, u16* __restrict__ xn,
                                               u16* __restrict__ w_inT, u16* __restrict__ w_outT) {
  __shared__ u16 tile[64][66];
  const int b = blockIdx.x, tid = threadIdx.x;
  if (b < 512) {
    const int row = b * 4 + (tid >> 6), lane = tid & 63;
    const u32x4 v = *(const u32x4*)(x + (size_t)row * D_ + lane * 8);
    float f[8] = {bflo(v.x), bfhi(v.x), bflo(v.y), bfhi(v.y), bflo(v.z), bfhi(v.z), bflo(v.w), bfhi(v.w)};
    float s = 0.f;
#pragma unroll
    for (int i = 0; i < 8; ++i) s += f[i];
#pragma unroll
    for (int o = 32; o >= 1; o >>= 1) s += __shfl_xor(s, o);
    const float m = s * (1.0f / D_);
    float q = 0.f;
#pragma unroll
    for (int i = 0; i < 8; ++i) { f[i] -= m; q += f[i] * f[i]; }
#pragma unroll
    for (int o = 32; o >= 1; o >>= 1) q += __shfl_xor(q, o);
    const float r = 1.0f / sqrtf(q * (1.0f / D_) + 1e-5f);
    u32x4 o4;
    o4.x = pk_bf16(f[0] * r, f[1] * r); o4.y = pk_bf16(f[2] * r, f[3] * r);
    o4.z = pk_bf16(f[4] * r, f[5] * r); o4.w = pk_bf16(f[6] * r, f[7] * r);
    *(u32x4*)(xn + (size_t)row * D_ + lane * 8) = o4;
  } else {
    int t = b - 512;
    const u16* src; u16* dst; int R, C, tr, tc;
    if (t < 224) { src = w_in; dst = w_inT; R = D_; C = F_; tr = t / 28; tc = t % 28; }
    else { t -= 224; src = w_out; dst = w_outT; R = FH; C = D_; tr = t / 8; tc = t % 8; }
#pragma unroll
    for (int it = 0; it < 2; ++it) {
      const int r = it * 32 + (tid >> 3), c = (tid & 7) * 8;
      const u32x4 v = *(const u32x4*)(src + (size_t)(tr * 64 + r) * C + tc * 64 + c);
      const unsigned w[4] = {v.x, v.y, v.z, v.w};
#pragma unroll
      for (int i = 0; i < 4; ++i) { tile[r][c + 2 * i] = (u16)(w[i] & 0xffff); tile[r][c + 2 * i + 1] = (u16)(w[i] >> 16); }
    }
    __syncthreads();
#pragma unroll
    for (int it = 0; it < 2; ++it) {
      const int c = it * 32 + (tid >> 3), r = (tid & 7) * 8;
      unsigned w[4];
#pragma unroll
      for (int i = 0; i < 4; ++i) w[i] = (unsigned)tile[r + 2 * i][c] | ((unsigned)tile[r + 2 * i + 1][c] << 16);
      *(u32x4*)(dst + (size_t)(tc * 64 + c) * R + tr * 64 + r) = (u32x4){w[0], w[1], w[2], w[3]};
    }
  }
}

using G1 = Gemm<128, 128, 2, 2, 2, 8>;
__device__ __forceinline__ float dpp_add16(float v) {
  v += __builtin_bit_cast(float, __builtin_amdgcn_update_dpp(0, __builtin_bit_cast(int, v), 0x128, 0xf, 0xf, false));
  v += __builtin_bit_cast(float, __builtin_amdgcn_update_dpp(0, __builtin_bit_cast(int, v), 0x124, 0xf, 0xf, false));
  v += __builtin_bit_cast(float, __builtin_amdgcn_update_dpp(0, __builtin_bit_cast(int, v), 0x122, 0xf, 0xf, false));
  v += __builtin_bit_cast(float, __builtin_amdgcn_update_dpp(0, __builtin_bit_cast(int, v), 0x121, 0xf, 0xf, false));
  return v;
}
__global__ __launch_bounds__(256, 2) void k1_abz(const u16* __restrict__ xn, const u16* __restrict__ wd_re,
                                                 const u16* __restrict__ wd_im, const u16* __restrict__ w_inT,
                                                 u16* __restrict__ aT, u16* __restrict__ bT, u16* __restrict__ z1,
                                                 u16* __restrict__ z2T, float* __restrict__ stats) {
  extern __shared__ __attribute__((aligned(16))) char smem[];
  LAS char* lds = (LAS char*)smem;
  const int w = __builtin_amdgcn_readfirstlane(threadIdx.x >> 6), lane = threadIdx.x & 63;
  const int b = blockIdx.x;
  const int pm = b & 15, pn = b >> 4;
  const u16* xrow = xn + (size_t)pm * 128 * D_;
  const u16* wrow = pn < 4 ? wd_re + (size_t)pn * 128 * D_ : pn < 8 ? wd_im + (size_t)(pn - 4) * 128 * D_ : w_inT + (size_t)(pn - 8) * 128 * D_;
  const bool isz1 = (pn >= 8 && pn < 15);
  f32x4 acc[G1::MT][G1::NT];
  G1::run(lds, isz1 ? wrow : xrow, D_ * 2, isz1 ? xrow : wrow, D_ * 2, acc, w, lane);
  const int wi = w >> 1, wj = w & 1, fr = lane & 15, fq = lane >> 4;
  if (pn < 8) {
    u16* o = (pn < 4 ? aT + (size_t)pn * 128 * S_ : bT + (size_t)(pn - 4) * 128 * S_) + pm * 128;
#pragma unroll
    for (int n = 0; n < 4; ++n)
#pragma unroll
      for (int h = 0; h < 2; ++h) {
        const f32x4 v0 = acc[2 * h][n], v1 = acc[2 * h + 1][n];
        u32x4 o4 = {pk_bf16(v0[0], v0[1]), pk_bf16(v0[2], v0[3]), pk_bf16(v1[0], v1[1]), pk_bf16(v1[2], v1[3])};
        *(u32x4*)(o + (size_t)(wj * 64 + n * 16 + fr) * S_ + wi * 64 + h * 32 + fq * 8) = o4;
      }
  } else if (isz1) {
    u16* o = z1 + (size_t)pm * 128 * FH + (pn - 8) * 128;
#pragma unroll
    for (int n = 0; n < 4; ++n)
#pragma unroll
      for (int h = 0; h < 2; ++h) {
        f32x4 v0 = acc[2 * h][n], v1 = acc[2 * h + 1][n];
#pragma unroll
        for (int r = 0; r < 4; ++r) { v0[r] = gelu_tanh(v0[r]); v1[r] = gelu_tanh(v1[r]); }
        u32x4 o4 = {pk_bf16(v0[0], v0[1]), pk_bf16(v0[2], v0[3]), pk_bf16(v1[0], v1[1]), pk_bf16(v1[2], v1[3])};
        *(u32x4*)(o + (size_t)(wj * 64 + n * 16 + fr) * FH + wi * 64 + h * 32 + fq * 8) = o4;
      }
  } else {
    const int tn = pn - 15;
    u16* o = z2T + (size_t)tn * 128 * S_ + pm * 128;
    float s1[2][8], s2[2][8];
#pragma unroll
    for (int h = 0; h < 2; ++h)
#pragma unroll
      for (int r = 0; r < 8; ++r) { s1[h][r] = 0.f; s2[h][r] = 0.f; }
#pragma unroll
    for (int n = 0; n < 4; ++n)
#pragma unroll
      for (int h = 0; h < 2; ++h) {
        f32x4 v0 = acc[2 * h][n], v1 = acc[2 * h + 1][n];
#pragma unroll
        for (int r = 0; r < 4; ++r) { v0[r] = gelu_tanh(v0[r]); v1[r] = gelu_tanh(v1[r]); }
        u32x4 o4 = {pk_bf16(v0[0], v0[1]), pk_bf16(v0[2], v0[3]), pk_bf16(v1[0], v1[1]), pk_bf16(v1[2], v1[3])};
        *(u32x4*)(o + (size_t)(wj * 64 + n * 16 + fr) * S_ + wi * 64 + h * 32 + fq * 8) = o4;
        const float g[8] = {bflo(o4.x), bfhi(o4.x), bflo(o4.y), bfhi(o4.y), bflo(o4.z), bfhi(o4.z), bflo(o4.w), bfhi(o4.w)};
#pragma unroll
        for (int r = 0; r < 8; ++r) { s1[h][r] += g[r]; s2[h][r] = __builtin_fmaf(g[r], g[r], s2[h][r]); }
      }
    float* st = stats + ((size_t)(tn * 2 + wj) * S_ + pm * 128 + wi * 64) * 2;
#pragma unroll
    for (int h = 0; h < 2; ++h)
#pragma unroll
      for (int r = 0; r < 8; ++r) {
        const float a = dpp_add16(s1[h][r]), c = dpp_add16(s2[h][r]);
        if (fr == h * 8 + r) { st[(h * 32 + fq * 8 + r) * 2] = a; st[(h * 32 + fq * 8 + r) * 2 + 1] = c; }
      }
  }
}

__global__ __launch_bounds__(256) void k15_norm(u16* __restrict__ z2T, const float* __restrict__ stats) {
  const int s0 = threadIdx.x * 8;
  float mean[8], rs[8];
#pragma unroll
  for (int i = 0; i < 8; ++i) { mean[i] = 0.f; rs[i] = 0.f; }
  for (int p = 0; p < 14; ++p) {
    const f32x4* sp = (const f32x4*)(stats + ((size_t)p * S_ + s0) * 2);
#pragma unroll
    for (int i = 0; i < 4; ++i) { const f32x4 v = sp[i]; mean[2 * i] += v[0]; rs[2 * i] += v[1]; mean[2 * i + 1] += v[2]; rs[2 * i + 1] += v[3]; }
  }
#pragma unroll
  for (int i = 0; i < 8; ++i) {
    const float m = mean[i] * (1.0f / FH);
    const float var = fmaxf(rs[i] * (1.0f / FH) - m * m, 0.f);
    mean[i] = m; rs[i] = 1.0f / sqrtf(var + 1e-5f);
  }
#pragma unroll
  for (int r = 0; r < 4; ++r) {
    u16* p = z2T + (size_t)(blockIdx.x * 4 + r) * S_ + s0;
    const u32x4 v = *(const u32x4*)p;
    const float f[8] = {bflo(v.x), bfhi(v.x), bflo(v.y), bfhi(v.y), bflo(v.z), bfhi(v.z), bflo(v.w), bfhi(v.w)};
    u32x4 o4;
    o4.x = pk_bf16((f[0] - mean[0]) * rs[0], (f[1] - mean[1]) * rs[1]);
    o4.y = pk_bf16((f[2] - mean[2]) * rs[2], (f[3] - mean[3]) * rs[3]);
    o4.z = pk_bf16((f[4] - mean[4]) * rs[4], (f[5] - mean[5]) * rs[5]);
    o4.w = pk_bf16((f[6] - mean[6]) * rs[6], (f[7] - mean[7]) * rs[7]);
    *(u32x4*)p = o4;
  }
}

using G2 = Gemm<128, 128, 2, 2, 4, 32>;
__global__ __launch_bounds__(256, 2) void k2_seq(const u16* __restrict__ ws_re, const u16* __restrict__ ws_im,
                                                 const u16* __restrict__ w_sp, const u16* __restrict__ aT,
                                                 const u16* __restrict__ bT, const u16* __restrict__ z2T,
                                                 const u16* __restrict__ z1, const u16* __restrict__ b_sp,
                                                 u16* __restrict__ yre, u16* __restrict__ yim, u16* __restrict__ hbuf) {
  extern __shared__ __attribute__((aligned(16))) char smem[];
  LAS char* lds = (LAS char*)smem;
  const int w = __builtin_amdgcn_readfirstlane(threadIdx.x >> 6), lane = threadIdx.x & 63;
  const int xcd = blockIdx.x & 7, idx = blockIdx.x >> 3;
  int type, pm, pn;
  if (xcd < 4) { type = xcd >> 1; pm = (xcd & 1) * 8 + (idx >> 2); pn = idx & 3; }
  else { if (idx >= 28) return; type = 2; pm = (xcd - 4) * 4 + idx / 7; pn = idx % 7; }
  const u16* Q = (type == 0 ? ws_re : type == 1 ? ws_im : w_sp) + (size_t)pm * 128 * S_;
  const u16* P = (type == 0 ? aT : type == 1 ? bT : z2T) + (size_t)pn * 128 * S_;
  f32x4 acc[G2::MT][G2::NT];
  G2::run(lds, P, S_ * 2, Q, S_ * 2, acc, w, lane);
  const int wi = w >> 1, wj = w & 1, fr = lane & 15, fq = lane >> 4;
  if (type < 2) {
    u16* o = (type == 0 ? yre : yim) + (size_t)pm * 128 * D_ + pn * 128;
#pragma unroll
    for (int n = 0; n < 4; ++n)
#pragma unroll
      for (int h = 0; h < 2; ++h) {
        const f32x4 v0 = acc[2 * h][n], v1 = acc[2 * h + 1][n];
        u32x4 o4 = {pk_bf16(v0[0], v0[1]), pk_bf16(v0[2], v0[3]), pk_bf16(v1[0], v1[1]), pk_bf16(v1[2], v1[3])};
        *(u32x4*)(o + (size_t)(wj * 64 + n * 16 + fr) * D_ + wi * 64 + h * 32 + fq * 8) = o4;
      }
  } else {
    const size_t base = (size_t)pm * 128 * FH + pn * 128;
#pragma unroll
    for (int n = 0; n < 4; ++n) {
      const int t = wj * 64 + n * 16 + fr;
      const float bs = bf2f(b_sp[pm * 128 + t]);
#pragma unroll
      for (int h = 0; h < 2; ++h) {
        const size_t off = base + (size_t)t * FH + wi * 64 + h * 32 + fq * 8;
        const u32x4 z = *(const u32x4*)(z1 + off);
        const f32x4 v0 = acc[2 * h][n], v1 = acc[2 * h + 1][n];
        u32x4 o4;
        o4.x = pk_bf16(bflo(z.x) * (v0[0] + bs), bfhi(z.x) * (v0[1] + bs));
        o4.y = pk_bf16(bflo(z.y) * (v0[2] + bs), bfhi(z.y) * (v0[3] + bs));
        o4.z = pk_bf16(bflo(z.z) * (v1[0] + bs), bfhi(z.z) * (v1[1] + bs));
        o4.w = pk_bf16(bflo(z.w) * (v1[2] + bs), bfhi(z.w) * (v1[3] + bs));
        *(u32x4*)(hbuf + off) = o4;
      }
    }
  }
}

using G3 = Gemm<64, 64, 2, 2, 8, 14>;
__global__ __launch_bounds__(256, 2) void k3_out(const u16* __restrict__ hbuf, const u16* __restrict__ w_outT,
                                                 const u16* __restrict__ x, const u16* __restrict__ yre,
                                                 const u16* __restrict__ yim, u16* __restrict__ out) {
  extern __shared__ __attribute__((aligned(16))) char smem[];
  LAS char* lds = (LAS char*)smem;
  const int w = __builtin_amdgcn_readfirstlane(threadIdx.x >> 6), lane = threadIdx.x & 63;
  const int xcd = blockIdx.x & 7, idx = blockIdx.x >> 3;
  const int pm = xcd * 4 + (idx >> 3), pn = idx & 7;
  f32x4 acc[G3::MT][G3::NT];
  G3::run(lds, w_outT + (size_t)pn * 64 * FH, FH * 2, hbuf + (size_t)pm * 64 * FH, FH * 2, acc, w, lane);
  const int wi = w >> 1, wj = w & 1, fr = lane & 15, fq = lane >> 4;
#pragma unroll
  for (int n = 0; n < 2; ++n) {
    const size_t off = (size_t)(pm * 64 + wj * 32 + n * 16 + fr) * D_ + pn * 64 + wi * 32 + fq * 8;
    const u32x4 xv = *(const u32x4*)(x + off), a = *(const u32x4*)(yre + off), b = *(const u32x4*)(yim + off);
    const f32x4 v0 = acc[0][n], v1 = acc[1][n];
    u32x4 o4;
    o4.x = pk_bf16(bflo(xv.x) + (bflo(a.x) - bflo(b.x)) + v0[0], bfhi(xv.x) + (bfhi(a.x) - bfhi(b.x)) + v0[1]);
    o4.y = pk_bf16(bflo(xv.y) + (bflo(a.y) - bflo(b.y)) + v0[2], bfhi(xv.y) + (bfhi(a.y) - bfhi(b.y)) + v0[3]);
    o4.z = pk_bf16(bflo(xv.z) + (bflo(a.z) - bflo(b.z)) + v1[0], bfhi(xv.z) + (bfhi(a.z) - bfhi(b.z)) + v1[1]);
    o4.w = pk_bf16(bflo(xv.w) + (bflo(a.w) - bflo(b.w)) + v1[2], bfhi(xv.w) + (bfhi(a.w) - bfhi(b.w)) + v1[3]);
    *(u32x4*)(out + off) = o4;
  }
}

extern "C" void kernel_launch(void* const* d_in, const int* in_sizes, int n_in, void* d_out, int out_size, void* d_ws,
                              size_t ws_size, hipStream_t stream) {
  const u16* x = (const u16*)d_in[0];
  const u16* ws_re = (const u16*)d_in[1];
  const u16* ws_im = (const u16*)d_in[2];
  const u16* wd_re = (const u16*)d_in[3];
  const u16* wd_im = (const u16*)d_in[4];
  const u16* w_in = (const u16*)d_in[6];
  const u16* w_sp = (const u16*)d_in[7];
  const u16* b_sp = (const u16*)d_in[8];
  const u16* w_out = (const u16*)d_in[9];
  u16* out = (u16*)d_out;
  char* ws = (char*)d_ws;
  size_t o = 0;
  auto carve = [&](size_t bytes) { char* p = ws + o; o += (bytes + 4095) & ~(size_t)4095; return p; };
  u16* xn = (u16*)carve((size_t)S_ * D_ * 2);
  u16* w_inT = (u16*)carve((size_t)F_ * D_ * 2);
  u16* w_outT = (u16*)carve((size_t)FH * D_ * 2);
  u16* aT = (u16*)carve((size_t)D_ * S_ * 2);
  u16* bT = (u16*)carve((size_t)D_ * S_ * 2);
  u16* z1 = (u16*)carve((size_t)S_ * FH * 2);
  u16* z2T = (u16*)carve((size_t)S_ * FH * 2);
  float* stats = (float*)carve((size_t)14 * S_ * 2 * 4);
  u16* yre = (u16*)carve((size_t)S_ * D_ * 2);
  u16* yim = (u16*)carve((size_t)S_ * D_ * 2);
  u16* hbuf = (u16*)carve((size_t)S_ * FH * 2);

  (void)hipFuncSetAttribute((const void*)k1_abz, hipFuncAttributeMaxDynamicSharedMemorySize, G1::LDS_BYTES);
  (void)hipFuncSetAttribute((const void*)k2_seq, hipFuncAttributeMaxDynamicSharedMemorySize, G2::LDS_BYTES);
  (void)hipFuncSetAttribute((const void*)k3_out, hipFuncAttributeMaxDynamicSharedMemorySize, G3::LDS_BYTES);

  k0_prep<<<512 + 224 + 112, 256, 0, stream>>>(x, w_in, w_out, xn, w_inT, w_outT);
  k1_abz<<<352, 256, G1::LDS_BYTES, stream>>>(xn, wd_re, wd_im, w_inT, aT, bT, z1, z2T, stats);
  k15_norm<<<224, 256, 0, stream>>>(z2T, stats);
  k2_seq<<<256, 256, G2::LDS_BYTES, stream>>>(ws_re, ws_im, w_sp, aT, bT, z2T, z1, b_sp, yre, yim, hbuf);
  k3_out<<<256, 256, G3::LDS_BYTES, stream>>>(hbuf, w_outT, x, yre, yim, out);
}
```

```cpp
#include <hip/hip_runtime.h>
#include <hip/hip_bf16.h>
#include <stdint.h>

typedef unsigned short u16;
typedef __bf16 bf16x8 __attribute__((ext_vector_type(8)));
typedef float f32x4 __attribute__((ext_vector_type(4)));
typedef float f32x2 __attribute__((ext_vector_type(2)));
typedef unsigned u32x4 __attribute__((ext_vector_type(4)));
typedef unsigned u32x2 __attribute__((ext_vector_type(2)));
typedef unsigned long long u64;
#define LAS __attribute__((address_space(3)))
#define GAS __attribute__((address_space(1)))
typedef __amdgpu_buffer_rsrc_t rsrc_t;

constexpr int S_ = 2048, D_ = 512, F_ = 1792, FH = 896;

constexpr size_t WS_EPOCH = 0;
constexpr size_t WS_STAT = 4096;
constexpr size_t WS_ROW = 512u << 10;
constexpr size_t WS_COL = 576u << 10;
constexpr size_t WS_CT = 1u << 20;
constexpr size_t WS_Z1 = WS_CT + (size_t)120 * 65536;
constexpr size_t WS_H = WS_Z1 + (size_t)128 * 28 * 1024;
constexpr size_t WS_Y = WS_H + (size_t)128 * 28 * 1024;

#define ST_AUX 16

#define NOST 0
#define ST_AUX1 16
#define STAMP(kern, i) do {} while (0)
template <int V> struct IC { static constexpr int value = V; };
template <int N, int I = 0, class F> __device__ __forceinline__ void static_for(F&& f) {
  if constexpr (I < N) { f(IC<I>{}); static_for<N, I + 1>(f); }
}

__device__ __forceinline__ float bflo(unsigned w) { return __uint_as_float(w << 16); }
__device__ __forceinline__ float bfhi(unsigned w) { return __uint_as_float(w & 0xffff0000u); }
__device__ __forceinline__ unsigned pk_bf16(float lo, float hi) {
  typedef __bf16 bf16x2 __attribute__((ext_vector_type(2)));
  f32x2 v = {lo, hi};
  return __builtin_bit_cast(unsigned, __builtin_convertvector(v, bf16x2));
}
__device__ __forceinline__ float gelu_tanh(float x) {
  const float c1 = -2.0f * 0.7978845608028654f * 1.4426950408889634f;
  const float c3 = c1 * 0.044715f;
  const float t = x * __builtin_fmaf(x * x, c3, c1);
  return x * __builtin_amdgcn_rcpf(1.0f + __builtin_amdgcn_exp2f(t));
}
__device__ __forceinline__ f32x2 gelu2(f32x2 x) {
  const float c1 = -2.0f * 0.7978845608028654f * 1.4426950408889634f;
  const float c3 = c1 * 0.044715f;
  const f32x2 t = x * __builtin_elementwise_fma(x * x, (f32x2){c3, c3}, (f32x2){c1, c1});
  f32x2 e = {__builtin_amdgcn_exp2f(t.x), __builtin_amdgcn_exp2f(t.y)};
  e = e + (f32x2){1.0f, 1.0f};
  return x * (f32x2){__builtin_amdgcn_rcpf(e.x), __builtin_amdgcn_rcpf(e.y)};
}
__device__ __forceinline__ f32x2 lo2(f32x4 v) { return (f32x2){v.x, v.y}; }
__device__ __forceinline__ f32x2 hi2(f32x4 v) { return (f32x2){v.z, v.w}; }
__device__ __forceinline__ unsigned pk2(f32x2 v) { return pk_bf16(v.x, v.y); }
__device__ __forceinline__ f32x2 up2(unsigned w) { return (f32x2){bflo(w), bfhi(w)}; }
__device__ __forceinline__ rsrc_t mk_rsrc(const void* p, unsigned bytes) {
  return __builtin_amdgcn_make_buffer_rsrc((void*)p, (short)0, (int)bytes, 0x00020000);
}
#define WAIT_V(n) asm volatile("s_waitcnt vmcnt(%0)" ::"n"(n) : "memory")
#define BAR() __builtin_amdgcn_s_barrier()
#define SCHED() __builtin_amdgcn_sched_barrier(0)
#define MEMFENCE() asm volatile("" ::: "memory")
#define WAIT_L0() do { asm volatile("s_waitcnt lgkmcnt(0)" ::: "memory"); SCHED(); } while (0)
#define DMA16(rs, ldsp, voff, soff) __builtin_amdgcn_raw_ptr_buffer_load_lds(rs, (LAS void*)(ldsp), 16, voff, soff, 0, 0)
#define TOUCH(rs, ldsp, voff, soff) __builtin_amdgcn_raw_ptr_buffer_load_lds(rs, (LAS void*)(ldsp), 4, voff, soff, 0, 0)
#define MFMA(a, b, c) __builtin_amdgcn_mfma_f32_16x16x32_bf16(a, b, c, 0, 0, 0)

template <int AUX> __device__ __forceinline__ void st16(u32x4 v, rsrc_t r, int off) { __builtin_amdgcn_raw_buffer_store_b128(v, r, off, 0, AUX); }
template <int OFF> __device__ __forceinline__ void ds128(bf16x8& d, unsigned a) {
  asm volatile("ds_read_b128 %0, %1 offset:%2" : "=v"(d) : "v"(a), "n"(OFF));
}
template <int OFF> __device__ __forceinline__ void dstr64(u32x2& d, unsigned a) {
  asm volatile("ds_read_b64_tr_b16 %0, %1 offset:%2" : "=v"(d) : "v"(a), "n"(OFF));
}
__device__ __forceinline__ bf16x8 join(const u32x2& lo, const u32x2& hi) {
  return __builtin_bit_cast(bf16x8, (u32x4){lo.x, lo.y, hi.x, hi.y});
}
__device__ __forceinline__ float dpp_add16(float v) {
  v += __builtin_bit_cast(float, __builtin_amdgcn_update_dpp(0, __builtin_bit_cast(int, v), 0x128, 0xf, 0xf, false));
  v += __builtin_bit_cast(float, __builtin_amdgcn_update_dpp(0, __builtin_bit_cast(int, v), 0x124, 0xf, 0xf, false));
  v += __builtin_bit_cast(float, __builtin_amdgcn_update_dpp(0, __builtin_bit_cast(int, v), 0x122, 0xf, 0xf, false));
  v += __builtin_bit_cast(float, __builtin_amdgcn_update_dpp(0, __builtin_bit_cast(int, v), 0x121, 0xf, 0xf, false));
  return v;
}

template <int CTRL> __device__ __forceinline__ float dpp_mov(float v) {
  return __builtin_bit_cast(float, __builtin_amdgcn_update_dpp(0, __builtin_bit_cast(int, v), CTRL, 0xf, 0xf, false));
}
__device__ __forceinline__ float reduce_scatter16(const float (&v)[16], int fr) {
  float a[8], b[4], c[2];
  const bool b3 = fr & 8, b2 = fr & 4, b1 = fr & 2, b0 = fr & 1;
#pragma unroll
  for (int i = 0; i < 8; ++i) a[i] = (b3 ? v[i + 8] : v[i]) + dpp_mov<0x128>(b3 ? v[i] : v[i + 8]);
#pragma unroll
  for (int i = 0; i < 4; ++i) b[i] = (b2 ? a[i + 4] : a[i]) + dpp_mov<0x141>(b2 ? a[i] : a[i + 4]);
#pragma unroll
  for (int i = 0; i < 2; ++i) c[i] = (b1 ? b[i + 2] : b[i]) + dpp_mov<0x4E>(b1 ? b[i] : b[i + 2]);
  return (b0 ? c[1] : c[0]) + dpp_mov<0xB1>(b0 ? c[0] : c[1]);
}
__device__ __forceinline__ float wave_sum(float s) { s = dpp_add16(s); s += __shfl_xor(s, 16); s += __shfl_xor(s, 32); return s; }

template <int NS, int NK, int L, int L0, int H0, int G, int XT, class Ord, class St, class Tail>
__device__ __forceinline__ void loader_ring(Ord&& ord, St&& st2, Tail&& tail) {
  static_assert(NS >= 3 && NK > NS, "ring");
  constexpr auto upto = [](int b) constexpr { int h = H0 + G * b, lim = b == 0 ? 2 * NS : 2 * (b - 1 + NS); h = h < lim ? h : lim; return h < 2 * NK ? h : 2 * NK; };
  constexpr auto cnt = [](int h) constexpr { return (h >> 1) * L + (h & 1) * L0 + (h == 2 * NK ? XT : 0); };
  constexpr int RAMP = []() constexpr { int b = 1; while (H0 + G * b < 2 * (b - 1 + NS)) ++b; return b; }();
  constexpr int B1 = RAMP < NK - NS + 1 ? RAMP : NK - NS + 1;
  auto seg = [&](auto B) {
    constexpr int b = B.value, h0 = b ? upto(b - 1) : 0, h1 = upto(b);
    static_for<h1 - h0>([&](auto H) { constexpr int h = h0 + H.value; st2((h >> 1) % NS, h >> 1, ord(h >> 1), IC<h & 1>{}); });
    if constexpr (h1 == 2 * NK && h0 < h1) tail();
    constexpr int n = cnt(h1) - (b + 1) * L;
    WAIT_V(n > 63 ? 63 : n);
    BAR();
    MEMFENCE();
  };
  static_for<B1 + 1>(seg);
  int buf = (B1 + NS - 1) % NS, kn = ord(B1 - 1 + NS);
#pragma unroll 1
  for (int b = B1 + 1; b <= NK - NS; ++b) {
    st2(buf, b - 2 + NS, kn, IC<0>{});
    st2(buf, b - 2 + NS, kn, IC<1>{});
    buf = buf + 1 == NS ? 0 : buf + 1;
    kn = ord(b - 1 + NS);
    asm volatile("" : "+s"(kn), "+s"(buf));
    WAIT_V((NS - 2) * L > 63 ? 63 : (NS - 2) * L);
    BAR();
    MEMFENCE();
  }
  constexpr int B2 = (B1 > NK - NS ? B1 : NK - NS) + 1;
  static_for<NK - B2>([&](auto B) { seg(IC<B2 + B.value>{}); });
}

template <int MODE> struct K1C {
  static constexpr int MT = MODE == 0 ? 4 : 2;
  static constexpr int NT = MODE == 1 ? 6 : 4;
  static constexpr int NWJ = MODE == 0 ? 4 : 2;
  static constexpr int NC = MODE == 0 ? 0 : MODE == 2 ? 1 : 2;
  static constexpr int W = NT * 16 * NWJ;
  static constexpr int NS = MODE == 2 ? 4 : 3;
  static constexpr int L = 4 + W / 32;
  static constexpr int WOFF = NS * 16384;
};
#define PFN 1
constexpr int K1_TAB = 147456;
constexpr int K1_LDS = K1_TAB + 8192;

__device__ __forceinline__ void gather4(u32x4 (&g)[4], const char* p0, const char* p1) {
  asm volatile("global_load_dwordx4 %0, %4, off sc1\n\tglobal_load_dwordx4 %1, %4, off offset:2048 sc1\n\t"
               "global_load_dwordx4 %2, %5, off sc1\n\tglobal_load_dwordx4 %3, %5, off offset:2048 sc1\n\ts_waitcnt vmcnt(0)"
               : "=&v"(g[0]), "=&v"(g[1]), "=&v"(g[2]), "=&v"(g[3]) : "v"(p0), "v"(p1) : "memory");
}
__device__ __forceinline__ u32x4 gather1(const char* p) {
  u32x4 g;
  asm volatile("global_load_dwordx4 %0, %1, off sc1\n\ts_waitcnt vmcnt(0)" : "=&v"(g) : "v"(p) : "memory");
  return g;
}

template <int MODE>
__device__ __forceinline__ void k1_loader(LAS char* lds, const u16* x, const u16* wsrc, char* ws, const u16* pf, int pfrows, int pm, int tile, int k0, int lw, int lane) {
  using C = K1C<MODE>;
  const rsrc_t rX = mk_rsrc(x + (size_t)pm * 128 * D_, 128 * D_ * 2);
  const int rho = 8 * lw + (lane >> 3);
  const int xrow = MODE == 1 ? rho : 8 * ((rho >> 2) & 3) + 4 * (rho >> 4) + (rho & 3);
  const unsigned sw = (unsigned)(((lane & 7) ^ ((rho >> 1) & 7)) << 4);
  const unsigned voffX = (unsigned)(xrow * (D_ * 2)) + sw;
  const rsrc_t rW = mk_rsrc(wsrc, MODE == 0 ? 256 * D_ * 2 : (D_ - 1) * F_ * 2 + C::W * 2);
  const int f = ((rho >> 1) & 1) | (((rho >> 3) & 1) << 1);
  const unsigned voffW = MODE == 0 ? (unsigned)(rho * (D_ * 2)) + sw
                                   : (unsigned)(rho * (F_ * 2) + (((((lane & 7) >> 1) ^ f) << 5) | ((lane & 1) << 4)));
  const int row = (lw & 1) * 64 + lane;
  const char* gp = ws + (lw < 2 || MODE != 0 ? WS_ROW + (size_t)pm * 2048 : WS_COL + (size_t)tile * 2048) + row * 16;
  u32x4 g;
  loader_ring<C::NS, 8, C::L, 4, 3, 3, 1 + PFN>([&](int t) { return (t + k0) & 7; }, [&](int buf, int t, int kk, auto PART) {
    if constexpr (PART.value == 0) {
      LAS char* p = lds + buf * 16384 + lw * 1024;
#pragma unroll
      for (int i = 0; i < 4; ++i) DMA16(rX, p + i * 4096, voffX, kk * 128 + i * 32 * D_ * 2);
    } else {
      LAS char* q = lds + C::WOFF + buf * 8192 + lw * 1024;
#pragma unroll
      for (int i = 0; i < C::W / 32; ++i)
        DMA16(rW, q + (i >> 1) * C::NS * 8192 + (i & 1) * 4096, voffW,
              MODE == 0 ? kk * 128 + i * 32 * D_ * 2 : (kk * 64 + (i & 1) * 32) * (F_ * 2) + (i >> 1) * 128);
    }
  }, [&]() {
    asm volatile("global_load_dwordx4 %0, %1, off sc1" : "=&v"(g) : "v"(gp) : "memory");
    const rsrc_t rF = mk_rsrc(pf, (unsigned)pfrows * (S_ * 2));
    const int fr8 = pfrows == 32 ? lw : lw & 1, fk = pfrows == 32 ? 0 : (lw >> 1) * PFN;
    const unsigned voff = (unsigned)((fr8 * 8 + (lane >> 3)) * (S_ * 2) + (lane & 7) * 128);
#pragma unroll
    for (int i = 0; i < PFN; ++i) TOUCH(rF, lds + K1_TAB + 7168, voff, (fk + i) * 1024);
  });
  asm volatile("s_barrier\n\ts_waitcnt vmcnt(%1)" : "+v"(g) : "n"(PFN) : "memory");
  if (lw < 2 || MODE == 0) {
    const u64 epoch = *(LAS u64*)(lds + K1_TAB + 6144 + (lw >> 1) * 8);
    const unsigned tagA = (unsigned)epoch + 1u, tagB = (unsigned)(epoch >> 32) + 0x9E3779B9u;
    for (int spin = 0; spin < (1 << 16); ++spin) {
      if (__all(g.y == tagA && g.w == tagB)) break;
      __builtin_amdgcn_s_sleep(2);
      g = gather1(gp);
    }
    ((LAS f32x2*)(lds + K1_TAB + (lw >> 1) * 1024))[row] = (f32x2){__uint_as_float(g.x), __uint_as_float(g.z)};
    asm volatile("s_waitcnt lgkmcnt(0)" ::: "memory");
  }
  BAR();
  if constexpr (MODE == 2) { BAR(); BAR(); }
}

template <int MODE>
__device__ __forceinline__ void k1_consumer(LAS char* lds, const u16* x, const u16* wsrc, char* ws, int pm, int tile, int ct  , int w, int lane) {
  using C = K1C<MODE>;
  constexpr int MT = C::MT, NT = C::NT, NS = C::NS, NC = C::NC, NWJ = C::NWJ;
  const int wi = w / NWJ, wj = w % NWJ, fr = lane & 15, fq = lane >> 4;
  const unsigned lb = (unsigned)(size_t)lds;
  STAMP(0, 0);
  const u64 epoch = __hip_atomic_load((GAS u64*)(ws + WS_EPOCH + pm * 128), __ATOMIC_RELAXED, __HIP_MEMORY_SCOPE_AGENT);
  u64 epoch2 = 0;
  u32x4 wa, wb;
  if constexpr (MODE == 0) {
    epoch2 = __hip_atomic_load((GAS u64*)(ws + WS_EPOCH + (16 + tile) * 128), __ATOMIC_RELAXED, __HIP_MEMORY_SCOPE_AGENT);
    wa = *(const u32x4*)(wsrc + (size_t)(pm * 16 + 2 * w) * D_ + lane * 8);
    wb = *(const u32x4*)(wsrc + (size_t)(pm * 16 + 2 * w + 1) * D_ + lane * 8);
  }
  const unsigned tagA = (unsigned)epoch + 1u, tagB = (unsigned)(epoch >> 32) + 0x9E3779B9u;
  const unsigned tagC = (unsigned)epoch2 + 1u, tagD = (unsigned)(epoch2 >> 32) + 0x9E3779B9u;
  {
    const int row = pm * 128 + tile * 8 + w;
    const u32x4 v = *(const u32x4*)(x + (size_t)row * D_ + lane * 8);
    f32x2 a = up2(v.x), b = up2(v.y), c = up2(v.z), d = up2(v.w);
    const f32x2 s2 = (a + b) + (c + d);
    const float mu = wave_sum(s2.x + s2.y) * (1.0f / D_);
    const f32x2 m2 = {mu, mu};
    a -= m2; b -= m2; c -= m2; d -= m2;
    const f32x2 q2 = (a * a + b * b) + (c * c + d * d);
    const float rstd = __builtin_amdgcn_rsqf(wave_sum(q2.x + q2.y) * (1.0f / D_) + 1e-5f);
    float sa = 0.f, sb = 0.f;
    if constexpr (MODE == 0) {
      const f32x2 ta = (up2(wa.x) + up2(wa.y)) + (up2(wa.z) + up2(wa.w)), tb = (up2(wb.x) + up2(wb.y)) + (up2(wb.z) + up2(wb.w));
      sa = wave_sum(ta.x + ta.y); sb = wave_sum(tb.x + tb.y);
    }
    if (lane == 0) {
      st16<16>((u32x4){__float_as_uint(mu), tagA, __float_as_uint(rstd), tagB}, mk_rsrc(ws + WS_ROW, S_ * 16), row * 16);
      if constexpr (MODE == 0) st16<16>((u32x4){__float_as_uint(sa), tagC, __float_as_uint(sb), tagD}, mk_rsrc(ws + WS_COL, 512 * 16), (tile * 128 + pm * 8 + w) * 16);
      if (w == 0) { *(LAS u64*)(lds + K1_TAB + 6144) = epoch; *(LAS u64*)(lds + K1_TAB + 6152) = epoch2; }
    }
  }
  const int rot = MODE == 0 ? 0 : MODE == 1 ? (wi == 3 ? 0 : 2 * wi) : wi;
  auto phys = [&](int n) { const int p = n + rot; return wj * NT + (p >= NT ? p - NT : p); };
  unsigned aX[2];
  unsigned aW[MODE == 0 ? 2 : NT];
#pragma unroll
  for (int ks = 0; ks < 2; ++ks) {
    aX[ks] = lb + (((wi * MT * 16 + fr) * 128 + ((fq ^ (fr >> 1)) << 4)) ^ (ks * 64));
    if constexpr (MODE == 0) aW[ks] = lb + C::WOFF + wj * NS * 8192 + ((fr * 128 + ((fq ^ (fr >> 1)) << 4)) ^ (ks * 64));
  }
  if constexpr (MODE != 0) {
    const int tq = fr >> 2, tp = fr & 3;
    const int tf = ((tq >> 1) & 1) | ((fq & 1) << 1);
#pragma unroll
    for (int n = 0; n < NT; ++n) {
      const int pn = phys(n);
      aW[n] = lb + C::WOFF + (pn >> 2) * NS * 8192 + (8 * fq + tq) * 128 + (((pn & 3) ^ tf) << 5) + tp * 8;
    }
  }
  f32x4 acc[MT][NT], cs[NC + 1];
#pragma unroll
  for (int m = 0; m < MT; ++m)
#pragma unroll
    for (int n = 0; n < NT; ++n) acc[m][n] = (f32x4){0.f, 0.f, 0.f, 0.f};
#pragma unroll
  for (int n = 0; n < NC; ++n) cs[n] = (f32x4){0.f, 0.f, 0.f, 0.f};
  bf16x8 ones = __builtin_bit_cast(bf16x8, (u32x4){0x3f803f80u, 0x3f803f80u, 0x3f803f80u, 0x3f803f80u});
  asm volatile("" : "+v"(ones));

  struct Frag { bf16x8 x[MT]; bf16x8 w[MODE == 0 ? NT : 1]; u32x2 t[MODE == 0 ? 1 : NT][2]; };
  Frag fa, fb;
  constexpr int NR = MT + (MODE == 0 ? NT : 2 * NT);
  constexpr int NM = MT * NT + NC;
  constexpr int NA = 2 + (MODE == 0 ? 2 : NT);
  auto rd = [&](Frag& f, auto I, auto KS) {
    constexpr int i = I.value, ks = KS.value;
    if constexpr (i < MT) ds128<i * 2048>(f.x[i], aX[ks]);
    else if constexpr (MODE == 0) { constexpr int n = i - MT; ds128<n * 2048>(f.w[n], aW[ks]); }
    else { constexpr int n = (i - MT) >> 1, t = (i - MT) & 1; dstr64<(32 * ks + 4 * t) * 128>(f.t[n][t], aW[n]); }
  };
  auto wfrag = [&](const Frag& f, int n) -> bf16x8 { if constexpr (MODE == 0) return f.w[n]; else return join(f.t[n][0], f.t[n][1]); };
  auto mm = [&](const Frag& f, auto I) {
    constexpr int i = I.value;
    if constexpr (i < MT * NT) {
      constexpr int n = i / MT, m = i % MT;
      if constexpr (MODE == 1) acc[m][n] = MFMA(wfrag(f, n), f.x[m], acc[m][n]);
      else acc[m][n] = MFMA(f.x[m], wfrag(f, n), acc[m][n]);
    } else { constexpr int n = i - MT * NT; cs[n] = MFMA(ones, wfrag(f, n), cs[n]); }
  };
  int dX = 16384, st = 0;
  auto half = [&](const Frag& f, Frag& g, auto KS) {
    constexpr int ks = KS.value;
    constexpr int RPS = (NR + NM - 3) / (NM - 2);
    static_for<NM>([&](auto I) {
      mm(f, I);
      SCHED();
      static_for<RPS>([&](auto R) { if constexpr (I.value * RPS + R.value < NR) { rd(g, IC<I.value * RPS + R.value>{}, KS); SCHED(); } });
      if constexpr (ks == 1 && I.value >= NM - 2) {
        static_for<NA>([&](auto U) {
          if constexpr ((U.value & 1) == I.value - (NM - 2)) {
            if constexpr (U.value < 2) aX[U.value] += dX; else aW[U.value - 2] += dX >> 1;
          }
        });
        SCHED();
      }
    });
    WAIT_L0();
  };
  asm volatile("s_waitcnt lgkmcnt(0)" ::: "memory");
  BAR();
  SCHED();
  STAMP(0, 1);
  static_for<NR>([&](auto I) { rd(fa, I, IC<0>{}); });
  WAIT_L0();
#pragma unroll 1
  for (int k = 0; k < 8; ++k) {
    const bool wrap = st == NS - 1;
    dX = wrap ? -(NS - 1) * 16384 : 16384;
    st = wrap ? 0 : st + 1;
    half(fa, fb, IC<1>{});
    BAR();
    SCHED();
    half(fb, fa, IC<0>{});
  }
  STAMP(0, 2);
  LAS f32x2* rowt = (LAS f32x2*)(lds + K1_TAB);
  LAS float* colt = (LAS float*)(lds + K1_TAB + 1024);
#pragma unroll
  for (int n = 0; n < NC; ++n)
    if (fq == 0) colt[phys(n) * 16 + fr] = cs[n][0];
  asm volatile("s_waitcnt lgkmcnt(0)" ::: "memory");
  BAR();
  MEMFENCE();
  STAMP(0, 7);
  if (tile == 0 && w == 0 && lane == 0)
    __hip_atomic_store((GAS u64*)(ws + WS_EPOCH + pm * 128), ((u64)tagB << 32) | tagA, __ATOMIC_RELAXED, __HIP_MEMORY_SCOPE_AGENT);
  if (MODE == 0 && pm == 0 && w == 0 && lane == 0)
    __hip_atomic_store((GAS u64*)(ws + WS_EPOCH + (16 + tile) * 128), ((u64)tagD << 32) | tagC, __ATOMIC_RELAXED, __HIP_MEMORY_SCOPE_AGENT);

  if constexpr (MODE == 1) {
    const rsrc_t rO = mk_rsrc(ws + WS_Z1 + (size_t)pm * 8 * 28 * 1024, 8 * 28 * 1024);
    f32x2 mr[MT];
#pragma unroll
    for (int m = 0; m < MT; ++m) mr[m] = rowt[(wi * MT + m) * 16 + fr];
#pragma unroll
    for (int h = 0; h < NT / 2; ++h) {
      const int pn = phys(2 * h);
      const f32x4 c0 = *(LAS f32x4*)(colt + pn * 16 + 4 * fq), c1 = *(LAS f32x4*)(colt + pn * 16 + 16 + 4 * fq);
#pragma unroll
      for (int m = 0; m < MT; ++m) {
        const f32x2 nm = {-mr[m].x, -mr[m].x}, rs = {mr[m].y, mr[m].y};
        const f32x4 v0 = acc[m][2 * h], v1 = acc[m][2 * h + 1];
        u32x4 o;
        o.x = pk2(gelu2(rs * __builtin_elementwise_fma(nm, lo2(c0), lo2(v0))));
        o.y = pk2(gelu2(rs * __builtin_elementwise_fma(nm, hi2(c0), hi2(v0))));
        o.z = pk2(gelu2(rs * __builtin_elementwise_fma(nm, lo2(c1), lo2(v1))));
        o.w = pk2(gelu2(rs * __builtin_elementwise_fma(nm, hi2(c1), hi2(v1))));
        if (!NOST || o.x == 0x12345u) st16<ST_AUX1>(o, rO, lane * 16 + ((wi * MT + m) * 28 + ((ct + pn) >> 1)) * 1024);
      }
    }
  } else {
    constexpr int NP = MT / 2;
    const rsrc_t rO = mk_rsrc(ws + WS_CT + (size_t)ct * 65536 + pm * 4096, (unsigned)(NWJ * NT) * 65536);
    f32x2 nmean[NP][4], rstd[NP][4];
#pragma unroll
    for (int h = 0; h < NP; ++h)
#pragma unroll
      for (int e = 0; e < 4; ++e) {
        const f32x4 t = *(LAS f32x4*)(rowt + wi * MT * 16 + 32 * h + 8 * fq + 2 * e);
        nmean[h][e] = (f32x2){-t.x, -t.z}; rstd[h][e] = (f32x2){t.y, t.w};
      }
    auto val = [&](int n, int h, int e, f32x2 c) {
      const f32x4 v = acc[2 * h + (e >> 1)][n];
      return rstd[h][e] * __builtin_elementwise_fma(nmean[h][e], c, (e & 1) ? hi2(v) : lo2(v));
    };
    if constexpr (MODE == 0) {
#pragma unroll
      for (int n = 0; n < NT; ++n) {
        const int pn = phys(n);
        const float c1 = colt[pn * 16 + fr];
        const f32x2 c = {c1, c1};
#pragma unroll
        for (int h = 0; h < NP; ++h) {
          const u32x4 o = {pk2(val(n, h, 0, c)), pk2(val(n, h, 1, c)), pk2(val(n, h, 2, c)), pk2(val(n, h, 3, c))};
          if (!NOST || o.x == 0x12345u) st16<ST_AUX1>(o, rO, lane * 16 + pn * 65536 + (wi * NP + h) * 1024);
        }
      }
    } else {
      const int zj = tile - 9;
      f32x2 z[NT][4], s1[4], s2[4];
#pragma unroll
      for (int n = 0; n < NT; ++n) {
        const float c1 = colt[phys(n) * 16 + fr];
        const f32x2 c = {c1, c1};
#pragma unroll
        for (int e = 0; e < 4; ++e) {
          const f32x2 g = gelu2(val(n, 0, e, c));
          z[n][e] = g;
          if (n == 0) { s1[e] = g; s2[e] = g * g; } else { s1[e] += g; s2[e] = __builtin_elementwise_fma(g, g, s2[e]); }
        }
      }
      LAS float* part = (LAS float*)(lds + K1_TAB + 2048);
      LAS f32x2* fin = (LAS f32x2*)(lds + K1_TAB + 4096);
      {
        const float v[16] = {s1[0].x, s2[0].x, s1[0].y, s2[0].y, s1[1].x, s2[1].x, s1[1].y, s2[1].y,
                             s1[2].x, s2[2].x, s1[2].y, s2[2].y, s1[3].x, s2[3].x, s1[3].y, s2[3].y};
        part[(wj * 128 + wi * 32 + 8 * fq) * 2 + fr] = reduce_scatter16(v, fr);
      }
      asm volatile("s_waitcnt lgkmcnt(0)" ::: "memory");
      BAR();
      MEMFENCE();
      STAMP(0, 3);
      if (w < 4) {
        const int row = (w & 1) * 64 + lane, hf = w >> 1;
        if (hf == 0) {
          const f32x2 p = ((LAS f32x2*)part)[row] + ((LAS f32x2*)part)[128 + row];
          st16<16>((u32x4){__float_as_uint(p.x), tagA, __float_as_uint(p.y), tagB}, mk_rsrc(ws + WS_STAT + (size_t)pm * 7 * 2048, 7 * 2048), row * 16 + zj * 2048);
        }
        float t1 = 0.f, t2 = 0.f;
        const char* gp = ws + WS_STAT + (size_t)pm * 7 * 2048 + hf * 3 * 2048 + row * 16;
        for (int spin = 0; spin < (1 << 16); ++spin) {
          u32x4 g[4];
          gather4(g, gp, gp + 4096);
          bool ok = true;
#pragma unroll
          for (int j = 0; j < 4; ++j) ok = ok && g[j].y == tagA && g[j].w == tagB;
          t1 = __uint_as_float(g[1].x) + __uint_as_float(g[2].x) + __uint_as_float(g[3].x) + (hf ? 0.f : __uint_as_float(g[0].x));
          t2 = __uint_as_float(g[1].z) + __uint_as_float(g[2].z) + __uint_as_float(g[3].z) + (hf ? 0.f : __uint_as_float(g[0].z));
          if (__all(ok)) break;
          __builtin_amdgcn_s_sleep(1);
        }
        fin[hf * 128 + row] = (f32x2){t1, t2};
      }
      asm volatile("s_waitcnt lgkmcnt(0)" ::: "memory");
      BAR();
      MEMFENCE();
      STAMP(0, 4);
#pragma unroll
      for (int e = 0; e < 4; ++e) {
        const f32x4 u = *(LAS f32x4*)(fin + wi * 32 + 8 * fq + 2 * e) + *(LAS f32x4*)(fin + 128 + wi * 32 + 8 * fq + 2 * e);
        const f32x2 mu = (f32x2){u.x, u.z} * (1.0f / FH);
        const f32x2 var = (f32x2){u.y, u.w} * (1.0f / FH) - mu * mu;
        nmean[0][e] = -mu;
        rstd[0][e] = (f32x2){__builtin_amdgcn_rsqf(fmaxf(var.x, 0.f) + 1e-5f), __builtin_amdgcn_rsqf(fmaxf(var.y, 0.f) + 1e-5f)};
      }
#pragma unroll
      for (int n = 0; n < NT; ++n) {
        u32x4 o;
#pragma unroll
        for (int e = 0; e < 4; ++e) o[e] = pk2((z[n][e] + nmean[0][e]) * rstd[0][e]);
        st16<ST_AUX>(o, rO, lane * 16 + phys(n) * 65536 + wi * 1024);
      }
    }
  }
  STAMP(0, 5);
  STAMP(0, 6);
}

__global__ __launch_bounds__(768, 3) void k1_abz(const u16* __restrict__ x, const u16* __restrict__ wd_re,
                                                 const u16* __restrict__ wd_im, const u16* __restrict__ w_in, char* ws,
                                                 const u16* __restrict__ ws_re, const u16* __restrict__ ws_im,
                                                 const u16* __restrict__ w_sp) {
#if defined(__HIP_DEVICE_COMPILE__)
  extern __shared__ __attribute__((aligned(16))) char smem[];
  LAS char* lds = (LAS char*)smem;
  const int w = __builtin_amdgcn_readfirstlane(threadIdx.x >> 6), lane = threadIdx.x & 63;
  const int xcd = blockIdx.x & 7, idx = blockIdx.x >> 3;
  const int lm = idx & 7, lt = idx >> 3;
  const int pm = (xcd & 1) * 8 + lm, tile = (xcd >> 1) + 4 * lt;
#define K0M 4
  const int k0 = K0M == 0 ? (lm + 2 * lt) & 7 : K0M == 1 ? (lm + lt) & 3 : K0M == 2 ? (lm + lt) & 1 : K0M == 3 ? 0 : K0M == 4 ? ((lm + lt) & 3) * 2 : ((lm + lt) & 1) * 4;
  const u16* pf = xcd < 4 ? (xcd < 2 ? ws_re : ws_im) + (size_t)((xcd & 1) * 1024 + idx * 32) * S_ : w_sp + (size_t)((xcd - 4) * 512 + idx * 16) * S_;
  const int pfrows = xcd < 4 ? 32 : 16;
  if (tile < 4) {
    const u16* wsrc = (tile < 2 ? wd_re : wd_im) + (size_t)(tile & 1) * 256 * D_;
    if (w >= 8) k1_loader<0>(lds, x, wsrc, ws, pf, pfrows, pm, tile, k0, w - 8, lane);
    else k1_consumer<0>(lds, x, wsrc, ws, pm, tile, tile * 16, w, lane);
  } else if (tile < 9) {
    const int c0 = tile < 8 ? (tile - 4) * 192 : FH - 192;
    if (w >= 8) k1_loader<1>(lds, x, w_in + c0, ws, pf, pfrows, pm, tile, k0, w - 8, lane);
    else k1_consumer<1>(lds, x, w_in, ws, pm, tile, c0 >> 4, w, lane);
  } else {
    if (w >= 8) k1_loader<2>(lds, x, w_in + FH + (tile - 9) * 128, ws, pf, pfrows, pm, tile, k0, w - 8, lane);
    else k1_consumer<2>(lds, x, w_in, ws, pm, tile, 64 + (tile - 9) * 8, w, lane);
  }
#endif
}

#define PF3 1
constexpr int K2_LDS = 5 * 32768;
__device__ __forceinline__ int korder32(int t, int j, int vl) {
  if (t < 1) return j;
  const int t1 = t - 1;
  if (t1 < 28) { const int u = t1 >> 2, v = t1 & 3; return v == vl ? 4 * (u + 1) + j : 4 * u + ((j + 1 + (v < vl ? v : v - 1)) & 3); }
  return 28 + ((j + 1 + (t1 - 28)) & 3);
}
__global__ __launch_bounds__(512, 2) void k2_seq(const u16* __restrict__ ws_re, const u16* __restrict__ ws_im,
                                                 const u16* __restrict__ w_sp, const u16* __restrict__ b_sp,
                                                 char* __restrict__ ws, const u16* __restrict__ w_out,
                                                 const u16* __restrict__ x) {
#if defined(__HIP_DEVICE_COMPILE__)
  extern __shared__ __attribute__((aligned(16))) char smem[];
  LAS char* lds = (LAS char*)smem;
  const int w = __builtin_amdgcn_readfirstlane(threadIdx.x >> 6), lane = threadIdx.x & 63;
  const int xcd = blockIdx.x & 7, idx = blockIdx.x >> 3;
  int type, pm, pn, lm;
  if (xcd < 4) { type = xcd >> 1; lm = idx >> 2; pm = (xcd & 1) * 8 + lm; pn = idx & 3; }
  else {
    if (idx >= 28) {
      if (w < 7) TOUCH(mk_rsrc(w_out, FH * D_ * 2), lds + w * 256, lane * 128, (((xcd - 4) * 4 + idx - 28) * 7 + w) * 8192);
      return;
    }
    type = 2; lm = idx / 7; pm = (xcd - 4) * 4 + lm; pn = idx % 7;
  }
  if (w >= 4) {
    const int lw = w - 4;
    const u16* Q = (type == 0 ? ws_re : type == 1 ? ws_im : w_sp) + (size_t)pm * 128 * S_;
    const rsrc_t rQ = mk_rsrc(Q, 128 * S_ * 2);
    const rsrc_t rP = mk_rsrc(ws + WS_CT + (size_t)(type * 32 + pn * 8) * 65536, 8 * 65536);
    const rsrc_t rZ = mk_rsrc(ws + WS_Z1 + (size_t)pm * 8 * 28 * 1024, 8 * 28 * 1024);
    const int j = (lm + pn) & 3;
    const int vl = (xcd < 4 ? (lm >> 2) * 2 + (pn & 1) : (pn >> 2) * 2 + (lm & 1)) & 3;
    const int rho = 8 * lw + (lane >> 3);
    const unsigned voffQ = (unsigned)(rho * (S_ * 2) + (((lane & 7) ^ ((rho >> 1) & 7)) << 4));
    const unsigned voffP = (unsigned)((lw >> 1) * 65536 + (lw & 1) * 1024 + lane * 16);
    loader_ring<4, 32, 8, 4, 3, 3, 0>([&](int t) { return korder32(t, j, vl); }, [&](int buf, int t, int kk, auto PART) {
      LAS char* p = lds + buf * 32768 + lw * 1024;
      if constexpr (PART.value == 1) {
#pragma unroll
        for (int i = 0; i < 4; ++i) DMA16(rQ, p + 16384 + i * 4096, voffQ, kk * 128 + i * 32 * S_ * 2);
        if (type == 2 && (t >> 3) == 1) DMA16(rZ, lds + 131072 + ((t & 7) * 4 + lw) * 1024, lane * 16, ((t & 7) * 28 + pn * 4 + lw) * 1024);
      } else {
#pragma unroll
        for (int i = 0; i < 4; ++i) DMA16(rP, p + i * 4096, voffP, kk * 2048 + i * 131072);
      }
    }, []() {});
    {
      const rsrc_t rW = mk_rsrc(w_out, FH * D_ * 2), rXx = mk_rsrc(x + (size_t)xcd * 256 * D_, 256 * D_ * 2);
      const int nw4 = xcd < 4 ? 128 : 112;
      for (int c = idx * 4 + lw; c < 144; c += nw4) {
        if (c < 112) TOUCH(rW, lds + 32768 + lw * 256, lane * 128, c * 8192);
        else TOUCH(rXx, lds + 32768 + lw * 256, lane * 128, (c - 112) * 8192);
      }
    }
    BAR();
    return;
  }
  STAMP(1, 0);
  const int wi = w >> 1, wj = w & 1, fr = lane & 15, fq = lane >> 4;
  const unsigned lb = (unsigned)(size_t)lds;
  unsigned aP[2], aQ[2][2];
#pragma unroll
  for (int sp = 0; sp < 2; ++sp) {
    aP[sp] = lb + sp * 65536 + wi * 8192 + lane * 16;
#pragma unroll
    for (int ks = 0; ks < 2; ++ks) aQ[sp][ks] = lb + sp * 65536 + 16384 + (((wj * 64 + fr) * 128 + ((fq ^ (fr >> 1)) << 4)) ^ (ks * 64));
  }
  f32x4 acc[4][4];
#pragma unroll
  for (int m = 0; m < 4; ++m)
#pragma unroll
    for (int n = 0; n < 4; ++n) acc[m][n] = (f32x4){0.f, 0.f, 0.f, 0.f};
  unsigned bsr[4];
#pragma unroll
  for (int n = 0; n < 4; ++n) bsr[n] = b_sp[pm * 128 + wj * 64 + n * 16 + fr];
  asm volatile("" : "+v"(bsr[0]), "+v"(bsr[1]), "+v"(bsr[2]), "+v"(bsr[3]));
  bf16x8 fa[8], fb[8];
  auto rd = [&](bf16x8 (&f)[8], auto I, auto S, auto KS) {
    constexpr int ord[8] = {0, 4, 5, 6, 7, 1, 2, 3};
    constexpr int x = ord[I.value], s = S.value, ks = KS.value;
    if constexpr (x < 4) ds128<(s & 1) * 32768 + (x * 2 + ks) * 1024>(f[x], aP[s >> 1]);
    else ds128<(s & 1) * 32768 + (x - 4) * 2048>(f[x], aQ[s >> 1][ks]);
  };
  auto half = [&](const bf16x8 (&f)[8], bf16x8 (&g)[8], auto S, auto KS, auto DOREAD) {
    static_for<16>([&](auto I) {
      constexpr int i = I.value;
      acc[i >> 2][i & 3] = MFMA(f[i >> 2], f[4 + (i & 3)], acc[i >> 2][i & 3]);
      SCHED();
      if constexpr (DOREAD.value && i < 8) { rd(g, I, S, KS); SCHED(); }
    });
    WAIT_L0();
  };
  BAR();
  SCHED();
  STAMP(1, 1);
  static_for<8>([&](auto I) { rd(fa, I, IC<0>{}, IC<0>{}); });
  WAIT_L0();
#pragma unroll 1
  for (int it = 0; it < 8; ++it) {
    static_for<4>([&](auto S) {
      half(fa, fb, S, IC<1>{}, IC<1>{});
      BAR();
      SCHED();
      half(fb, fa, IC<(S.value + 1) & 3>{}, IC<0>{}, IC<1>{});
    });
  }
  STAMP(1, 2);
  if (type < 2) {
    const rsrc_t rO = mk_rsrc(ws + WS_Y + ((size_t)type * 128 + pm * 8) * 16 * 1024, 8 * 16 * 1024);
#pragma unroll
    for (int n = 0; n < 4; ++n)
#pragma unroll
      for (int h = 0; h < 2; ++h) {
        const f32x4 v0 = acc[2 * h][n], v1 = acc[2 * h + 1][n];
        const u32x4 o = {pk_bf16(v0[0], v0[1]), pk_bf16(v0[2], v0[3]), pk_bf16(v1[0], v1[1]), pk_bf16(v1[2], v1[3])};
        st16<ST_AUX>(o, rO, lane * 16 + ((wj * 4 + n) * 16 + pn * 4 + wi * 2 + h) * 1024);
      }
  } else {
    const rsrc_t rO = mk_rsrc(ws + WS_H + (size_t)pm * 8 * 28 * 1024, 8 * 28 * 1024);
#pragma unroll
    for (int n = 0; n < 4; ++n)
#pragma unroll
      for (int h = 0; h < 2; ++h) {
        const f32x4 v0 = acc[2 * h][n], v1 = acc[2 * h + 1][n];
        const u32x4 zz = *(LAS u32x4*)(lds + 131072 + ((wj * 4 + n) * 4 + wi * 2 + h) * 1024 + lane * 16);
        const float b1 = bflo(bsr[n]);
        const f32x2 b = {b1, b1};
        u32x4 o;
        o.x = pk2(up2(zz.x) * (lo2(v0) + b));
        o.y = pk2(up2(zz.y) * (hi2(v0) + b));
        o.z = pk2(up2(zz.z) * (lo2(v1) + b));
        o.w = pk2(up2(zz.w) * (hi2(v1) + b));
        st16<ST_AUX>(o, rO, lane * 16 + ((wj * 4 + n) * 28 + pn * 4 + wi * 2 + h) * 1024);
      }
  }
  STAMP(1, 5);
  STAMP(1, 6);
#endif
}

#define K3_H0 6
#define K3_G 4
constexpr int K3_NS = 7, K3_LDS = K3_NS * 16384;
__global__ __launch_bounds__(512, 2) void k3_out(const u16* __restrict__ w_out, const u16* __restrict__ x,
                                                 const char* __restrict__ ws, u16* __restrict__ out) {
#if defined(__HIP_DEVICE_COMPILE__)
  extern __shared__ __attribute__((aligned(16))) char smem[];
  LAS char* lds = (LAS char*)smem;
  const int w = __builtin_amdgcn_readfirstlane(threadIdx.x >> 6), lane = threadIdx.x & 63;
  const int xcd = blockIdx.x & 7, idx = blockIdx.x >> 3;
  const int lm = idx >> 3, pm = xcd * 4 + lm, pn = idx & 7;
  if (w >= 4) {
    const int lw = w - 4;
    const rsrc_t rW = mk_rsrc(w_out + pn * 64, (FH - 1) * D_ * 2 + 128);
    const rsrc_t rH = mk_rsrc(ws + WS_H + (size_t)pm * 4 * 28 * 1024, 4 * 28 * 1024);
    const int rho = 8 * lw + (lane >> 3);
    const unsigned voffW = (unsigned)(rho * (D_ * 2) + (((((lane & 7) >> 1) ^ ((rho >> 1) & 3)) << 5) | ((lane & 1) << 4)));
    const unsigned voffH = (unsigned)((lw >> 1) * 28 * 1024 + (lw & 1) * 1024 + lane * 16);
    const int k0 = (pn * 7 / 4 + lm * 7 / 2) % 14;
    loader_ring<K3_NS, 14, 4, 2, K3_H0, K3_G, 0>([&](int t) { return t + k0 >= 14 ? t + k0 - 14 : t + k0; }, [&](int buf, int t, int kk, auto PART) {
      LAS char* p = lds + buf * 16384 + lw * 1024;
      if constexpr (PART.value == 0) {
#pragma unroll
        for (int i = 0; i < 2; ++i) DMA16(rW, p + i * 4096, voffW, (kk * 64 + i * 32) * (D_ * 2));
      } else {
#pragma unroll
        for (int i = 0; i < 2; ++i) DMA16(rH, p + 8192 + i * 4096, voffH, kk * 2048 + i * 2 * 28 * 1024);
      }
    }, []() {});
    return;
  }
  const int wi = w >> 1, wj = w & 1, fr = lane & 15, fq = lane >> 4;
  const unsigned lb = (unsigned)(size_t)lds;
  STAMP(2, 0);
  const rsrc_t rY = mk_rsrc(ws + WS_Y, 2 * 128 * 16 * 1024);
  u32x4 yr[2], yi[2];
  u32x2 xv[2][2];
#pragma unroll
  for (int n = 0; n < 2; ++n) {
    const int tt = pm * 4 + wj * 2 + n;
    yr[n] = __builtin_amdgcn_raw_buffer_load_b128(rY, lane * 16, (tt * 16 + pn * 2 + wi) * 1024, 0);
    yi[n] = __builtin_amdgcn_raw_buffer_load_b128(rY, lane * 16, ((128 + tt) * 16 + pn * 2 + wi) * 1024, 0);
#pragma unroll
    for (int a = 0; a < 2; ++a) xv[n][a] = *(const u32x2*)(x + (size_t)(tt * 16 + fr) * D_ + pn * 64 + wi * 32 + a * 16 + 4 * fq);
  }
  unsigned aW[2][2], aH[2];
  {
    const int tq = fr >> 2, tp = fr & 3, row = 4 * fq + tq;
#pragma unroll
    for (int sp = 0; sp < 2; ++sp) {
#pragma unroll
      for (int m = 0; m < 2; ++m) aW[sp][m] = lb + sp * 65536 + row * 128 + (((wi * 2 + m) ^ ((row >> 1) & 3)) << 5) + tp * 8;
      aH[sp] = lb + sp * 65536 + 8192 + wj * 4096 + lane * 16;
    }
  }
  f32x4 acc[2][2];
#pragma unroll
  for (int m = 0; m < 2; ++m)
#pragma unroll
    for (int n = 0; n < 2; ++n) acc[m][n] = (f32x4){0.f, 0.f, 0.f, 0.f};
  struct Frag { u32x2 p[2][2][2]; bf16x8 q[2][2]; };
  Frag fa, fb;
  auto rdall = [&](Frag& f, auto S) {
    constexpr int s = S.value, so = (s & 3) * 16384;
    static_for<2>([&](auto KS) {
      constexpr int ks = KS.value;
      static_for<2>([&](auto M) {
        dstr64<so + (32 * ks) * 128>(f.p[ks][M.value][0], aW[s >> 2][M.value]);
        dstr64<so + (32 * ks + 16) * 128>(f.p[ks][M.value][1], aW[s >> 2][M.value]);
      });
      static_for<2>([&](auto N) { ds128<so + (N.value * 2 + ks) * 1024>(f.q[ks][N.value], aH[s >> 2]); });
    });
    SCHED();
  };
  auto mma = [&](const Frag& f) {
#pragma unroll
    for (int ks = 0; ks < 2; ++ks)
#pragma unroll
      for (int m = 0; m < 2; ++m)
#pragma unroll
        for (int n = 0; n < 2; ++n) acc[m][n] = MFMA(join(f.p[ks][m][0], f.p[ks][m][1]), f.q[ks][n], acc[m][n]);
    SCHED();
  };
  BAR();
  SCHED();
  STAMP(2, 1);
  rdall(fa, IC<0>{});
  WAIT_L0();
  static_for<7>([&](auto KP) {
    constexpr int k = 2 * KP.value;
    BAR(); SCHED();
    rdall(fb, IC<(k + 1) % K3_NS>{});
    mma(fa);
    WAIT_L0();
    if constexpr (k + 2 < 14) { BAR(); SCHED(); rdall(fa, IC<(k + 2) % K3_NS>{}); }
    mma(fb);
    WAIT_L0();
  });
  STAMP(2, 2);
#pragma unroll
  for (int n = 0; n < 2; ++n) {
    const int tt = pm * 4 + wj * 2 + n;
    const unsigned a4[4] = {yr[n].x, yr[n].y, yr[n].z, yr[n].w}, b4[4] = {yi[n].x, yi[n].y, yi[n].z, yi[n].w};
#pragma unroll
    for (int a = 0; a < 2; ++a) {
      const f32x4 v = acc[a][n];
      u32x2 o;
      o.x = pk_bf16(bflo(xv[n][a].x) + (bflo(a4[2 * a]) - bflo(b4[2 * a])) + v[0], bfhi(xv[n][a].x) + (bfhi(a4[2 * a]) - bfhi(b4[2 * a])) + v[1]);
      o.y = pk_bf16(bflo(xv[n][a].y) + (bflo(a4[2 * a + 1]) - bflo(b4[2 * a + 1])) + v[2], bfhi(xv[n][a].y) + (bfhi(a4[2 * a + 1]) - bfhi(b4[2 * a + 1])) + v[3]);
      *(u32x2*)(out + (size_t)(tt * 16 + fr) * D_ + pn * 64 + wi * 32 + a * 16 + 4 * fq) = o;
    }
  }
  STAMP(2, 5);
  STAMP(2, 6);
#endif
}

extern "C" void kernel_launch(void* const* d_in, const int* in_sizes, int n_in, void* d_out, int out_size, void* d_ws,
                              size_t ws_size, hipStream_t stream) {
  const u16* x = (const u16*)d_in[0];
  const u16* ws_re = (const u16*)d_in[1];
  const u16* ws_im = (const u16*)d_in[2];
  const u16* wd_re = (const u16*)d_in[3];
  const u16* wd_im = (const u16*)d_in[4];
  const u16* w_in = (const u16*)d_in[6];
  const u16* w_sp = (const u16*)d_in[7];
  const u16* b_sp = (const u16*)d_in[8];
  const u16* w_out = (const u16*)d_in[9];
  char* ws = (char*)d_ws;
  (void)hipFuncSetAttribute((const void*)k1_abz, hipFuncAttributeMaxDynamicSharedMemorySize, K1_LDS);
  (void)hipFuncSetAttribute((const void*)k2_seq, hipFuncAttributeMaxDynamicSharedMemorySize, K2_LDS);
  (void)hipFuncSetAttribute((const void*)k3_out, hipFuncAttributeMaxDynamicSharedMemorySize, K3_LDS);
  k1_abz<<<256, 768, K1_LDS, stream>>>(x, wd_re, wd_im, w_in, ws, ws_re, ws_im, w_sp);
  k2_seq<<<256, 512, K2_LDS, stream>>>(ws_re, ws_im, w_sp, b_sp, ws, w_out, x);
  k3_out<<<256, 512, K3_LDS, stream>>>(w_out, x, ws, (u16*)d_out);
}
```

```cpp
#include <hip/hip_runtime.h>
#include <hip/hip_bf16.h>
#include <stdint.h>

typedef unsigned short u16;
typedef __bf16 bf16x8 __attribute__((ext_vector_type(8)));
typedef float f32x4 __attribute__((ext_vector_type(4)));
typedef float f32x2 __attribute__((ext_vector_type(2)));
typedef unsigned u32x4 __attribute__((ext_vector_type(4)));
typedef unsigned u32x2 __attribute__((ext_vector_type(2)));
typedef unsigned long long u64;
#define LAS __attribute__((address_space(3)))
#define GAS __attribute__((address_space(1)))
typedef __amdgpu_buffer_rsrc_t rsrc_t;

constexpr int S_ = 2048, D_ = 512, F_ = 1792, FH = 896;

constexpr size_t WS_EPOCH = 0;
constexpr size_t WS_STAT = 4096;
constexpr size_t WS_ROW = 512u << 10;
constexpr size_t WS_COL = 576u << 10;
constexpr size_t WS_FLG = 640u << 10;
constexpr size_t WS_FL3 = 672u << 10;
constexpr size_t WS_CT = 1u << 20;
constexpr size_t WS_Z1 = WS_CT + (size_t)120 * 65536;
constexpr size_t WS_H = WS_Z1 + (size_t)128 * 28 * 1024;
constexpr size_t WS_Y = WS_H + (size_t)128 * 28 * 1024;

#define ST_AUX 16

#define STAMP(kern, i) do {} while (0)
template <int V> struct IC { static constexpr int value = V; };
template <int N, int I = 0, class F> __device__ __forceinline__ void static_for(F&& f) {
  if constexpr (I < N) { f(IC<I>{}); static_for<N, I + 1>(f); }
}

__device__ __forceinline__ float bflo(unsigned w) { return __uint_as_float(w << 16); }
__device__ __forceinline__ float bfhi(unsigned w) { return __uint_as_float(w & 0xffff0000u); }
__device__ __forceinline__ unsigned pk_bf16(float lo, float hi) {
  typedef __bf16 bf16x2 __attribute__((ext_vector_type(2)));
  f32x2 v = {lo, hi};
  return __builtin_bit_cast(unsigned, __builtin_convertvector(v, bf16x2));
}
__device__ __forceinline__ float gelu_tanh(float x) {
  const float c1 = -2.0f * 0.7978845608028654f * 1.4426950408889634f;
  const float c3 = c1 * 0.044715f;
  const float t = x * __builtin_fmaf(x * x, c3, c1);
  return x * __builtin_amdgcn_rcpf(1.0f + __builtin_amdgcn_exp2f(t));
}
__device__ __forceinline__ f32x2 gelu2(f32x2 x) {
  const float c1 = -2.0f * 0.7978845608028654f * 1.4426950408889634f;
  const float c3 = c1 * 0.044715f;
  const f32x2 t = x * __builtin_elementwise_fma(x * x, (f32x2){c3, c3}, (f32x2){c1, c1});
  f32x2 e = {__builtin_amdgcn_exp2f(t.x), __builtin_amdgcn_exp2f(t.y)};
  e = e + (f32x2){1.0f, 1.0f};
  return x * (f32x2){__builtin_amdgcn_rcpf(e.x), __builtin_amdgcn_rcpf(e.y)};
}
__device__ __forceinline__ f32x2 lo2(f32x4 v) { return (f32x2){v.x, v.y}; }
__device__ __forceinline__ f32x2 hi2(f32x4 v) { return (f32x2){v.z, v.w}; }
__device__ __forceinline__ unsigned pk2(f32x2 v) { return pk_bf16(v.x, v.y); }
__device__ __forceinline__ f32x2 up2(unsigned w) { return (f32x2){bflo(w), bfhi(w)}; }
__device__ __forceinline__ rsrc_t mk_rsrc(const void* p, unsigned bytes) {
  return __builtin_amdgcn_make_buffer_rsrc((void*)p, (short)0, (int)bytes, 0x00020000);
}
#define WAIT_V(n) asm volatile("s_waitcnt vmcnt(%0)" ::"n"(n) : "memory")
#define BAR() __builtin_amdgcn_s_barrier()
#define SCHED() __builtin_amdgcn_sched_barrier(0)
#define MEMFENCE() asm volatile("" ::: "memory")
#define WAIT_L0() do { asm volatile("s_waitcnt lgkmcnt(0)" ::: "memory"); SCHED(); } while (0)
#define DMA16(rs, ldsp, voff, soff) __builtin_amdgcn_raw_ptr_buffer_load_lds(rs, (LAS void*)(ldsp), 16, voff, soff, 0, 0)
#define DMA16D(rs, ldsp, voff, soff) __builtin_amdgcn_raw_ptr_buffer_load_lds(rs, (LAS void*)(ldsp), 16, voff, soff, 0, 16)
#define TOUCH(rs, ldsp, voff, soff) __builtin_amdgcn_raw_ptr_buffer_load_lds(rs, (LAS void*)(ldsp), 4, voff, soff, 0, 0)
#define MFMA(a, b, c) __builtin_amdgcn_mfma_f32_16x16x32_bf16(a, b, c, 0, 0, 0)

template <int AUX> __device__ __forceinline__ void st16(u32x4 v, rsrc_t r, int off) { __builtin_amdgcn_raw_buffer_store_b128(v, r, off, 0, AUX); }
template <int OFF> __device__ __forceinline__ void ds128(bf16x8& d, unsigned a) {
  asm volatile("ds_read_b128 %0, %1 offset:%2" : "=v"(d) : "v"(a), "n"(OFF));
}
template <int OFF> __device__ __forceinline__ void dstr64(u32x2& d, unsigned a) {
  asm volatile("ds_read_b64_tr_b16 %0, %1 offset:%2" : "=v"(d) : "v"(a), "n"(OFF));
}
__device__ __forceinline__ bf16x8 join(const u32x2& lo, const u32x2& hi) {
  return __builtin_bit_cast(bf16x8, (u32x4){lo.x, lo.y, hi.x, hi.y});
}
__device__ __forceinline__ float dpp_add16(float v) {
  v += __builtin_bit_cast(float, __builtin_amdgcn_update_dpp(0, __builtin_bit_cast(int, v), 0x128, 0xf, 0xf, false));
  v += __builtin_bit_cast(float, __builtin_amdgcn_update_dpp(0, __builtin_bit_cast(int, v), 0x124, 0xf, 0xf, false));
  v += __builtin_bit_cast(float, __builtin_amdgcn_update_dpp(0, __builtin_bit_cast(int, v), 0x122, 0xf, 0xf, false));
  v += __builtin_bit_cast(float, __builtin_amdgcn_update_dpp(0, __builtin_bit_cast(int, v), 0x121, 0xf, 0xf, false));
  return v;
}

template <int CTRL> __device__ __forceinline__ float dpp_mov(float v) {
  return __builtin_bit_cast(float, __builtin_amdgcn_update_dpp(0, __builtin_bit_cast(int, v), CTRL, 0xf, 0xf, false));
}
__device__ __forceinline__ float reduce_scatter16(const float (&v)[16], int fr) {
  float a[8], b[4], c[2];
  const bool b3 = fr & 8, b2 = fr & 4, b1 = fr & 2, b0 = fr & 1;
#pragma unroll
  for (int i = 0; i < 8; ++i) a[i] = (b3 ? v[i + 8] : v[i]) + dpp_mov<0x128>(b3 ? v[i] : v[i + 8]);
#pragma unroll
  for (int i = 0; i < 4; ++i) b[i] = (b2 ? a[i + 4] : a[i]) + dpp_mov<0x141>(b2 ? a[i] : a[i + 4]);
#pragma unroll
  for (int i = 0; i < 2; ++i) c[i] = (b1 ? b[i + 2] : b[i]) + dpp_mov<0x4E>(b1 ? b[i] : b[i + 2]);
  return (b0 ? c[1] : c[0]) + dpp_mov<0xB1>(b0 ? c[0] : c[1]);
}
__device__ __forceinline__ float wave_sum(float s) { s = dpp_add16(s); s += __shfl_xor(s, 16); s += __shfl_xor(s, 32); return s; }

constexpr int A_TAB = 131072;
constexpr int T_ROW = A_TAB, T_COL = A_TAB + 1024, T_PART = A_TAB + 2048, T_FIN = A_TAB + 4096, T_EPO = A_TAB + 6144, T_JUNK = A_TAB + 7168;
constexpr int A_LDS = A_TAB + 32768;
constexpr int SPIN = 1 << 16;
#define KMID 4
#define KPUB 2
#define SPQ 64
#define SDELAY 16
#define FDELAY 24
#define PSLEEP 1
#define QDELAY 12

struct Tags { unsigned a, b; };
__device__ __forceinline__ Tags next_tags(u64 e) { return Tags{(unsigned)e + 1u, (unsigned)(e >> 32) + 0x9E3779B9u}; }
__device__ __forceinline__ u64 tag64(Tags t) { return ((u64)t.b << 32) | t.a; }
__device__ __forceinline__ u64 load_epoch(char* ws) { return __hip_atomic_load((GAS u64*)(ws + WS_EPOCH), __ATOMIC_RELAXED, __HIP_MEMORY_SCOPE_AGENT); }
__device__ __forceinline__ void ld16_issue(u32x4& g, const char* p) { asm volatile("global_load_dwordx4 %0, %1, off sc1" : "=&v"(g) : "v"(p) : "memory"); }
__device__ __forceinline__ u32x4 ld16_now(const char* p) {
  u32x4 g;
  asm volatile("global_load_dwordx4 %0, %1, off sc1\n\ts_waitcnt vmcnt(0)" : "=&v"(g) : "v"(p) : "memory");
  return g;
}
__device__ __forceinline__ u32x2 ld8_now(const char* p) {
  u32x2 g;
  asm volatile("global_load_dwordx2 %0, %1, off sc1\n\ts_waitcnt vmcnt(0)" : "=&v"(g) : "v"(p) : "memory");
  return g;
}
__device__ __forceinline__ int korder32(int t, int j, int vl) {
  if (t < 1) return j;
  const int t1 = t - 1;
  if (t1 < 28) { const int u = t1 >> 2, v = t1 & 3; return v == vl ? 4 * (u + 1) + j : 4 * u + ((j + 1 + (v < vl ? v : v - 1)) & 3); }
  return 28 + ((j + 1 + (t1 - 28)) & 3);
}
__device__ __forceinline__ void load_row4(u32x4 (&v)[4], const u16* rows, int lane) {
#pragma unroll
  for (int i = 0; i < 4; ++i) v[i] = *(const u32x4*)(rows + (size_t)(lane >> 4) * D_ + i * 128 + (lane & 15) * 8);
}
__device__ __forceinline__ float row4_sum(const u32x4 (&v)[4]) {
  f32x2 s = {0.f, 0.f};
#pragma unroll
  for (int i = 0; i < 4; ++i) s += (up2(v[i].x) + up2(v[i].y)) + (up2(v[i].z) + up2(v[i].w));
  return dpp_add16(s.x + s.y);
}

template <int MODE, class Hook>
__device__ __forceinline__ void p1_loop(LAS char* lds, f32x4 (&acc)[4][4], int w, int lane, Hook&& hook) {
  const int wi = w >> 1, wj = w & 1, fr = lane & 15, fq = lane >> 4;
  const unsigned lb = (unsigned)(size_t)lds;
  constexpr int NC = MODE == 0 ? 0 : 2, NAW = MODE == 0 ? 2 : 4;
  unsigned aX[2], aW[NAW];
#pragma unroll
  for (int ks = 0; ks < 2; ++ks) {
    aX[ks] = lb + (((wi * 64 + fr) * 128 + ((fq ^ (fr >> 1)) << 4)) ^ (ks * 64));
    if constexpr (MODE == 0) aW[ks] = lb + 16384 + (((wj * 64 + fr) * 128 + ((fq ^ (fr >> 1)) << 4)) ^ (ks * 64));
  }
  if constexpr (MODE != 0) {
    const int tq = fr >> 2, tp = fr & 3, tf = ((tq >> 1) & 1) | ((fq & 1) << 1);
#pragma unroll
    for (int n = 0; n < 4; ++n) aW[n] = lb + 16384 + wj * 8192 + (8 * fq + tq) * 128 + ((n ^ (2 * wi) ^ tf) << 5) + tp * 8;
  }
  f32x4 cs[2];
#pragma unroll
  for (int m = 0; m < 4; ++m)
#pragma unroll
    for (int n = 0; n < 4; ++n) acc[m][n] = (f32x4){0.f, 0.f, 0.f, 0.f};
  cs[0] = (f32x4){0.f, 0.f, 0.f, 0.f}; cs[1] = cs[0];
  bf16x8 ones = __builtin_bit_cast(bf16x8, (u32x4){0x3f803f80u, 0x3f803f80u, 0x3f803f80u, 0x3f803f80u});
  asm volatile("" : "+v"(ones));
  struct Frag { bf16x8 x[4]; bf16x8 w[MODE == 0 ? 4 : 1]; u32x2 t[MODE == 0 ? 1 : 4][2]; };
  Frag fa, fb;
  constexpr int NR = MODE == 0 ? 8 : 12, NM = 16 + NC;
  auto rd = [&](Frag& f, auto I, auto KS) {
    constexpr int i = I.value, ks = KS.value;
    if constexpr (i < 4) ds128<i * 2048>(f.x[i], aX[ks]);
    else if constexpr (MODE == 0) ds128<(i - 4) * 2048>(f.w[i - 4], aW[ks]);
    else { constexpr int n = (i - 4) >> 1, t = (i - 4) & 1; dstr64<(32 * ks + 4 * t) * 128>(f.t[n][t], aW[n]); }
  };
  auto wfrag = [&](const Frag& f, int n) -> bf16x8 { if constexpr (MODE == 0) return f.w[n]; else return join(f.t[n][0], f.t[n][1]); };
  int dS = 32768;
  auto half = [&](const Frag& f, Frag& g, auto KS) {
    static_for<NM>([&](auto I) {
      constexpr int i = I.value;
      if constexpr (i < 16) {
        constexpr int n = i >> 2, m = i & 3;
        if constexpr (MODE == 1) acc[m][n] = MFMA(wfrag(f, n), f.x[m], acc[m][n]);
        else acc[m][n] = MFMA(f.x[m], wfrag(f, n), acc[m][n]);
      } else cs[i - 16] = MFMA(ones, wfrag(f, i - 16), cs[i - 16]);
      SCHED();
      if constexpr (i < NR) { rd(g, I, KS); SCHED(); }
      else if constexpr (KS.value == 1 && i - NR < 2 + NAW) {
        if constexpr (i - NR < 2) aX[i - NR] += dS; else aW[i - NR - 2] += dS;
        SCHED();
      }
    });
    WAIT_L0();
  };
  static_for<NR>([&](auto I) { rd(fa, I, IC<0>{}); });
  WAIT_L0();
#pragma unroll 1
  for (int k = 0; k < 8; ++k) {
    dS = (k & 3) == 3 ? -98304 : 32768;
    half(fa, fb, IC<1>{});
    BAR();
    SCHED();
    half(fb, fa, IC<0>{});
    hook(k);
    SCHED();
  }
  if constexpr (NC > 0) {
    LAS float* colt = (LAS float*)(lds + T_COL);
    if (fq == 0) { colt[wj * 64 + wi * 32 + fr] = cs[0][0]; colt[wj * 64 + wi * 32 + 16 + fr] = cs[1][0]; }
  }
  asm volatile("s_waitcnt lgkmcnt(0)" ::: "memory");
}

template <class Hook>
__device__ __forceinline__ void p2_loop(LAS char* lds, f32x4 (&acc)[4][4], int w, int lane, char* ws, Hook&& hook) {
  const int wi = w >> 1, wj = w & 1, fr = lane & 15, fq = lane >> 4;
  const unsigned lb = (unsigned)(size_t)lds;
  unsigned aP[2], aQ[2][2];
#pragma unroll
  for (int sp = 0; sp < 2; ++sp) {
    aP[sp] = lb + sp * 65536 + wi * 8192 + lane * 16;
#pragma unroll
    for (int ks = 0; ks < 2; ++ks) aQ[sp][ks] = lb + sp * 65536 + 16384 + (((wj * 64 + fr) * 128 + ((fq ^ (fr >> 1)) << 4)) ^ (ks * 64));
  }
#pragma unroll
  for (int m = 0; m < 4; ++m)
#pragma unroll
    for (int n = 0; n < 4; ++n) acc[m][n] = (f32x4){0.f, 0.f, 0.f, 0.f};
  bf16x8 fa[8], fb[8];
  auto rd = [&](bf16x8 (&f)[8], auto I, auto S, auto KS) {
    constexpr int x = I.value, s = S.value, ks = KS.value;
    if constexpr (x < 4) ds128<(s & 1) * 32768 + (x * 2 + ks) * 1024>(f[x], aP[s >> 1]);
    else ds128<(s & 1) * 32768 + (x - 4) * 2048>(f[x], aQ[s >> 1][ks]);
  };
  auto half = [&](const bf16x8 (&f)[8], bf16x8 (&g)[8], auto S, auto KS) {
    static_for<16>([&](auto I) {
      constexpr int i = I.value;
      acc[i >> 2][i & 3] = MFMA(f[i >> 2], f[4 + (i & 3)], acc[i >> 2][i & 3]);
      SCHED();
      if constexpr (i < 8) { rd(g, I, S, KS); SCHED(); }
    });
    WAIT_L0();
  };
  BAR();
  SCHED();
  STAMP(1, 1);
  static_for<8>([&](auto I) { rd(fa, I, IC<0>{}, IC<0>{}); });
  WAIT_L0();
#pragma unroll 1
  for (int it = 0; it < 8; ++it) {
    static_for<4>([&](auto S) {
      half(fa, fb, S, IC<1>{});
      BAR();
      SCHED();
      half(fb, fa, IC<(S.value + 1) & 3>{}, IC<0>{});
    });
    hook(it);
    SCHED();
  }
}

struct RowLN { f32x2 nm[2][4], rs[2][4]; };
__device__ __forceinline__ void read_rowln(RowLN& r, LAS char* lds, int wi, int fq) {
#pragma unroll
  for (int h = 0; h < 2; ++h)
#pragma unroll
    for (int e = 0; e < 4; ++e) {
      const f32x4 t = *(LAS f32x4*)(lds + T_ROW + (wi * 64 + 32 * h + 8 * fq + 2 * e) * 8);
      r.nm[h][e] = (f32x2){-t.x, -t.z}; r.rs[h][e] = (f32x2){t.y, t.w};
    }
}
__device__ __forceinline__ f32x2 ln_val(const RowLN& r, const f32x4 (&acc)[4][4], int n, int h, int e, f32x2 c) {
  const f32x4 v = acc[2 * h + (e >> 1)][n];
  return r.rs[h][e] * __builtin_elementwise_fma(r.nm[h][e], c, (e & 1) ? hi2(v) : lo2(v));
}
__device__ __forceinline__ void publish_at(char* p, int lane, Tags tg) {
  WAIT_V(0);
  if (lane == 0) __hip_atomic_store((GAS u64*)p, tag64(tg), __ATOMIC_RELAXED, __HIP_MEMORY_SCOPE_AGENT);
}
__device__ __forceinline__ void publish_flag(char* ws, int c, int pm, int w, int lane, Tags tg) {
  publish_at(ws + WS_FLG + (size_t)(c * 64 + pm * 4 + w) * 8, lane, tg);
}
__device__ __forceinline__ void wait_flags(const char* fp, Tags tg) {
#pragma unroll 1
  for (int spin = 0; spin < SPIN; ++spin) {
    const u32x2 fl = ld8_now(fp);
    if (__all(fl.x == tg.a && fl.y == tg.b)) break;
    __builtin_amdgcn_s_sleep(PSLEEP);
  }
}

struct Ring2 {
  rsrc_t rQ, rP; unsigned voffQ, voffP; LAS char* lw_lds; int j, vl;
  __device__ __forceinline__ void Q(int buf, int kk) const {
#pragma unroll
    for (int i = 0; i < 4; ++i) DMA16(rQ, lw_lds + buf * 32768 + 16384 + i * 4096, voffQ, kk * 128 + i * 32 * S_ * 2);
  }
  __device__ __forceinline__ void P(int buf, int kk) const {
#pragma unroll
    for (int i = 0; i < 4; ++i) DMA16D(rP, lw_lds + buf * 32768 + i * 4096, voffP, kk * 2048 + i * 131072);
  }
  __device__ __forceinline__ int ord(int t) const { return korder32(t, j, vl); }
};

template <bool G>
__device__ __forceinline__ Tags p1_loader(LAS char* lds, char* ws, const u16* x, const u16* wd, const u16* win, const Ring2& r2,
                                          int lm, int pm, int pn, int c, int lw, int lane) {
  constexpr int NK = G ? 8 : 16;
  const int rho = 8 * lw + (lane >> 3);
  const unsigned sw = (unsigned)(((lane & 7) ^ ((rho >> 1) & 7)) << 4);
  const rsrc_t rX = mk_rsrc(x + (size_t)pm * 128 * D_, 128 * D_ * 2);
  const unsigned voffXp = (unsigned)((8 * ((rho >> 2) & 3) + 4 * (rho >> 4) + (rho & 3)) * (D_ * 2)) + sw;
  const unsigned voffXn = (unsigned)(rho * (D_ * 2)) + sw;
  const rsrc_t rW = mk_rsrc(win, (D_ - 1) * F_ * 2 + 256), rD = mk_rsrc(wd, 128 * D_ * 2);
  const int f = ((rho >> 1) & 1) | (((rho >> 3) & 1) << 1);
  const unsigned voffW = (unsigned)(rho * (F_ * 2) + (((((lane & 7) >> 1) ^ f) << 5) | ((lane & 1) << 4)));
  const int k0 = G ? (2 * lm + pn) & 7 : (lm + 2 * pn) & 7;
  LAS char* lw_lds = lds + lw * 1024;
  auto p1part = [&](auto H) {
    constexpr int h = H.value, t = h >> 1;
    const int kk = (t + k0) & 7;
    LAS char* p = lw_lds + (t & 3) * 32768;
    if constexpr ((h & 1) == 0) {
#pragma unroll
      for (int i = 0; i < 4; ++i) DMA16(rX, p + i * 4096, t < 8 ? voffXp : voffXn, kk * 128 + i * 32 * D_ * 2);
    } else if constexpr (!G && t < 8) {
#pragma unroll
      for (int i = 0; i < 4; ++i) DMA16(rD, p + 16384 + i * 4096, voffXn, kk * 128 + i * 32 * D_ * 2);
    } else {
#pragma unroll
      for (int i = 0; i < 4; ++i) DMA16(rW, p + 16384 + i * 4096, voffW, (kk * 64 + (i & 1) * 32) * (F_ * 2) + (i >> 1) * 128);
    }
  };
  constexpr int BG = G ? 6 : 5;
  constexpr auto upto = [](int b) constexpr { int h = 3 + 3 * b, lim = b == 0 ? 8 : 2 * (b + 3); h = h < lim ? h : lim; return h < 2 * NK ? h : 2 * NK; };
  constexpr auto after = [upto](int hx, int b) constexpr {
    int n = 0;
    for (int s = 0; s <= b; ++s) {
      for (int h = s ? upto(s - 1) : 0; h < upto(s); ++h) n = h == hx ? 0 : n + 4;
      n += s == BG ? 1 : 0;
      if (!G && s >= 14) n += 4;
    }
    return n;
  };
  const int row = (lw & 1) * 64 + lane, hf = lw >> 1;
  const char* gp = ws + (G || lw < 2 ? WS_ROW + (size_t)(pm * 128 + row) * 16 : WS_COL + (size_t)(c * 64 + lane) * 16);
  u32x4 g;
  Tags tg = {0u, 0u};
  auto table1 = [&]() {
    asm volatile("" : "+v"(g));
    tg = next_tags(*(LAS u64*)(lds + T_EPO));
    if (lw < (G ? 2 : 3)) {
#pragma unroll 1
      for (int spin = 0; spin < SPIN; ++spin) {
        if (__all(g.y == tg.a && g.w == tg.b)) break;
        __builtin_amdgcn_s_sleep(2);
        g = ld16_now(gp);
      }
      *(LAS f32x2*)(lds + (lw < 2 ? T_ROW + row * 8 : T_COL + lane * 8)) = (f32x2){__uint_as_float(g.x), __uint_as_float(g.z)};
      asm volatile("s_waitcnt lgkmcnt(0)" ::: "memory");
    }
  };
  static_for<NK>([&](auto B) {
    constexpr int b = B.value, h0 = b ? upto(b - 1) : 0, h1 = upto(b);
    static_for<h1 - h0>([&](auto H) { p1part(IC<h0 + H.value>{}); });
    if constexpr (b == BG) ld16_issue(g, gp);
    if constexpr (!G && b == 9) {
      WAIT_V(32);
      table1();
      BAR();
      WAIT_V(after(17, 9));
      BAR();
    }
    if constexpr (!G && b >= 14) r2.Q(b - 14, r2.ord(b - 14));
    if constexpr (!(!G && b == 8)) WAIT_V(after(2 * b + 1, b));
    BAR();
    MEMFENCE();
  });
  if constexpr (G) {
    BAR();
    WAIT_V(0);
    table1();
    BAR();
    r2.Q(0, r2.ord(0)); r2.Q(1, r2.ord(1)); r2.Q(2, r2.ord(2)); r2.Q(3, r2.ord(3));
    BAR();
    const char* sp = ws + WS_STAT + (size_t)pm * 7 * 2048 + hf * 3 * 2048 + row * 16;
    u32x4 q[4];
    __builtin_amdgcn_s_sleep(SDELAY);
#pragma unroll 1
    for (int spin = 0; spin < SPIN; ++spin) {
#pragma unroll
      for (int i = 0; i < 4; ++i) ld16_issue(q[i], sp + i * 2048);
      asm volatile("s_waitcnt vmcnt(0)" : "+v"(q[0]), "+v"(q[1]), "+v"(q[2]), "+v"(q[3]) :: "memory");
      bool ok = true;
#pragma unroll
      for (int i = 0; i < 4; ++i) ok = ok && q[i].y == tg.a && q[i].w == tg.b;
      if (__all(ok)) break;
    }
    const float t1 = __uint_as_float(q[1].x) + __uint_as_float(q[2].x) + __uint_as_float(q[3].x) + (hf ? 0.f : __uint_as_float(q[0].x));
    const float t2 = __uint_as_float(q[1].z) + __uint_as_float(q[2].z) + __uint_as_float(q[3].z) + (hf ? 0.f : __uint_as_float(q[0].z));
    *(LAS f32x2*)(lds + T_FIN + (hf * 128 + row) * 8) = (f32x2){t1, t2};
    asm volatile("s_waitcnt lgkmcnt(0)" ::: "memory");
    BAR();
    __builtin_amdgcn_s_sleep(FDELAY);
  } else {
    r2.Q(2, r2.ord(2));
    BAR();
    r2.Q(3, r2.ord(3));
    BAR();
  }
  return tg;
}

__global__ __launch_bounds__(512, 2) void fm_main(const u16* __restrict__ x, const u16* __restrict__ wd_re,
                                                  const u16* __restrict__ wd_im, const u16* __restrict__ w_in,
                                                  const u16* __restrict__ ws_re, const u16* __restrict__ ws_im,
                                                  const u16* __restrict__ w_sp, const u16* __restrict__ b_sp,
                                                  const u16* __restrict__ w_out, char* ws, u16* __restrict__ out) {
#if defined(__HIP_DEVICE_COMPILE__)
  extern __shared__ __attribute__((aligned(16))) char smem[];
  LAS char* lds = (LAS char*)smem;
  const int w = __builtin_amdgcn_readfirstlane(threadIdx.x >> 6), lane = threadIdx.x & 63;
  const int xcd = blockIdx.x & 7, idx = blockIdx.x >> 3;
  const int wi = (w >> 1) & 1, wj = w & 1, fr = lane & 15, fq = lane >> 4;
  const bool isY = xcd < 4, spare = !isY && idx >= 28;
  const int type = xcd >> 1;
  const int lm = isY ? idx >> 2 : spare ? idx - 28 : idx / 7;
  const int pm = isY ? (xcd & 1) * 8 + lm : (xcd - 4) * 4 + lm;
  const int pn = isY ? idx & 3 : spare ? 7 : idx % 7;
  const int c = isY ? type * 4 + pn : 8 + pn;
  const int zt = c < 7 ? c : 6;
  const int hb = isY ? 1 : 0, on = isY ? c : pn;
  {
    u64 pc; int len1, len2;
    asm volatile("s_getpc_b64 %0\n.Lfm_pc:\n\ts_mov_b32 %1, .Lfm_e1-.Lfm_pc\n\ts_mov_b32 %2, .Lfm_e2-.Lfm_pc" : "=s"(pc), "=s"(len1), "=s"(len2));
    const unsigned len = (unsigned)(len1 > len2 ? len1 : len2);
    if (w == 3 && len < 65536u && (unsigned)idx * 8192u < len) TOUCH(mk_rsrc((const void*)pc, len), lds + T_JUNK, lane * 128, idx * 8192);
  }
  Tags tg = {0u, 0u};
  if (spare) {
    const int sid = pm;
    tg = next_tags(load_epoch(ws));
    const int r0 = sid * 64 + w * 8;
    const u16* src = (r0 < 512 ? wd_re : wd_im) + (size_t)(r0 & 511) * D_;
    u32x4 va[4], vb[4];
#pragma unroll
    for (int i = 0; i < 4; ++i) {
      va[i] = *(const u32x4*)(src + (size_t)(2 * fq) * D_ + i * 128 + fr * 8);
      vb[i] = *(const u32x4*)(src + (size_t)(2 * fq + 1) * D_ + i * 128 + fr * 8);
    }
    const float sa = row4_sum(va), sb = row4_sum(vb);
    if (fr == 0) st16<ST_AUX>((u32x4){__float_as_uint(sa), tg.a, __float_as_uint(sb), tg.b}, mk_rsrc(ws + WS_COL, 512 * 16), (r0 / 2 + fq) * 16);
    MEMFENCE();
    BAR();
    MEMFENCE();
    {
      const rsrc_t rA = mk_rsrc(w_in, D_ * F_ * 2), rB = mk_rsrc(w_sp + (size_t)(xcd - 4) * 512 * S_, 512 * S_ * 2), rC = mk_rsrc(w_out, FH * D_ * 2);
      if (w < 7) {
        const int id = (sid * 7 + w) * 64 + lane;
        TOUCH(rA, lds + T_JUNK, (id / 14) * (F_ * 2) + (id % 14) * 128, 0);
      }
#pragma unroll 1
      for (int ch = w; ch < SPQ; ch += 8) {
        const int id = (ch * 4 + lm) * 64 + lane;
        TOUCH(rB, lds + T_JUNK, ((id >> 1) & 511) * (S_ * 2) + (2 * (id >> 10) + (id & 1)) * 128, 0);
        if (ch == w + 16 && w < 7) TOUCH(rC, lds + T_JUNK, lane * 128, (sid * 7 + w) * 8192);
      }
    }
    if (sid == 0 && w == 0) {
#pragma unroll 1
      for (int spin = 0; spin < SPIN; ++spin) {
        u32x4 g[16];
#pragma unroll
        for (int i = 0; i < 16; ++i)
          ld16_issue(g[i], i < 8 ? ws + WS_COL + (size_t)(i * 64 + lane) * 16 : ws + WS_FLG + (size_t)(i < 15 ? (i - 8) * 64 + lane : 7 * 64 + (lane & 31)) * 16);
        WAIT_V(0);
        bool ok = true;
#pragma unroll
        for (int i = 0; i < 16; ++i) {
          asm volatile("" : "+v"(g[i]));
          ok = ok && (i < 8 ? (g[i].y == tg.a && g[i].w == tg.b) : (g[i].x == tg.a && g[i].y == tg.b && g[i].z == tg.a && g[i].w == tg.b));
        }
        if (__all(ok)) break;
        __builtin_amdgcn_s_sleep(32);
      }
      if (lane == 0) __hip_atomic_store((GAS u64*)(ws + WS_EPOCH), tag64(tg), __ATOMIC_RELAXED, __HIP_MEMORY_SCOPE_AGENT);
    }
    WAIT_V(0);
  }

  if (w >= 4) {
    const int lw = w - 4, rho = 8 * lw + (lane >> 3);
    LAS char* lw_lds = lds + lw * 1024;
    const rsrc_t rW = mk_rsrc(w_out + on * 64, (FH - 1) * D_ * 2 + 128);
    const unsigned voffW = (unsigned)(rho * (D_ * 2) + (((((lane & 7) >> 1) ^ ((rho >> 1) & 3)) << 5) | ((lane & 1) << 4)));
    const int k3 = on < 7 ? on : 0;
    auto ord3 = [&](int t) { return t + k3 >= 7 ? t + k3 - 7 : t + k3; };
    auto issW = [&](int t0, int t1) {
#pragma unroll 1
      for (int t = t0; t < t1; ++t) {
        const int kk = ord3(t);
#pragma unroll
        for (int i = 0; i < 4; ++i) DMA16(rW, lw_lds + t * 16384 + i * 4096, voffW, (kk * 128 + i * 32) * (D_ * 2));
      }
    };
    if (!spare) {
      Ring2 r2;
      r2.rQ = mk_rsrc((isY ? (type ? ws_im : ws_re) : w_sp) + (size_t)pm * 128 * S_, 128 * S_ * 2);
      r2.rP = mk_rsrc(ws + WS_CT + (size_t)c * 8 * 65536, 8 * 65536);
      r2.voffQ = (unsigned)(rho * (S_ * 2) + (((lane & 7) ^ ((rho >> 1) & 7)) << 4));
      r2.voffP = (unsigned)((lw >> 1) * 65536 + (lw & 1) * 1024 + lane * 16);
      r2.lw_lds = lw_lds;
      r2.j = (lm + pn) & 3;
      r2.vl = (isY ? (lm >> 2) * 2 + (pn & 1) : (pn >> 2) * 2 + (lm & 1)) & 3;
      if (isY) tg = p1_loader<false>(lds, ws, x, (type ? wd_im : wd_re) + (size_t)pn * 128 * D_, w_in + zt * 128, r2, lm, pm, pn, c, lw, lane);
      else tg = p1_loader<true>(lds, ws, x, wd_re, w_in + FH + pn * 128, r2, lm, pm, pn, c, lw, lane);
      if (lw == 0) {
        wait_flags(ws + WS_FLG + (size_t)(c * 64 + lane) * 8, tg);
        *(volatile LAS unsigned*)(lds + T_EPO + 8) = 1u;
      } else {
#pragma unroll 1
        for (int spin = 0; spin < (SPIN << 4) && __builtin_amdgcn_readfirstlane(*(volatile LAS unsigned*)(lds + T_EPO + 8)) == 0u; ++spin) __builtin_amdgcn_s_sleep(1);
      }
      MEMFENCE();
      r2.P(0, r2.ord(0)); r2.P(1, r2.ord(1));
      WAIT_V(4); BAR();
      r2.P(2, r2.ord(2)); r2.P(3, r2.ord(3));
      WAIT_V(8); BAR(); MEMFENCE();
      { const int kk = r2.ord(4); r2.P(0, kk); r2.Q(0, kk); }
      WAIT_V(12); BAR(); MEMFENCE();
      int buf = 1, kn = r2.ord(5);
      const rsrc_t rZ = mk_rsrc(ws + WS_Z1 + (size_t)pm * 8 * 28 * 1024, 8 * 28 * 1024);
#pragma unroll 1
      for (int t = 5; t < 32; ++t) {
        r2.P(buf, kn); r2.Q(buf, kn);
        if (!isY && (t >> 3) == 2) {
          if (t == 16) wait_flags(ws + WS_FLG + (size_t)((15 + pn) * 64 + pm * 4 + (lane & 3)) * 8, tg);
          DMA16D(rZ, lds + A_TAB + ((t & 7) * 4 + lw) * 1024, lane * 16, ((t & 7) * 28 + pn * 4 + lw) * 1024);
        }
        buf = (buf + 1) & 3;
        kn = r2.ord(t + 1);
        asm volatile("" : "+s"(kn), "+s"(buf));
        WAIT_V(16); BAR(); MEMFENCE();
      }
      WAIT_V(8); BAR(); MEMFENCE();
      issW(0, 4);
      WAIT_V(16); BAR();
      BAR();
    } else issW(0, 4);
    BAR();
    MEMFENCE();
    issW(4, 7);
    WAIT_V(0);
    BAR();
    if (lw == 0) {
      const int l = lane & 15;
      if (spare) __builtin_amdgcn_s_sleep(127);
      wait_flags(ws + WS_FL3 + (size_t)((pm * 2 + hb) * 32 + (l < 14 ? l : 16 + (l & 1) * 8 + on)) * 8, tg);
    }
    BAR();
    MEMFENCE();
    const rsrc_t rH = mk_rsrc(ws + WS_H + (size_t)(pm * 8 + hb * 4 + lw) * 28 * 1024, 28 * 1024);
    static_for<7>([&](auto B) {
      constexpr int b = B.value, s0 = 2 * b < 7 ? 2 * b : 7, s1 = 2 * b + 2 < 7 ? 2 * b + 2 : 7;
      static_for<s1 - s0>([&](auto T) {
        const int kk = ord3(s0 + T.value);
#pragma unroll
        for (int i = 0; i < 4; ++i) DMA16D(rH, lds + (s0 + T.value) * 16384 + lw * 4096 + i * 1024, lane * 16, kk * 4096 + i * 1024);
      });
      WAIT_V(4 * (s1 - b - 1));
      BAR();
    });
    asm volatile(".Lfm_e1:");
    return;
  }

  if (!spare) {
    f32x4 acc[4][4];
    unsigned bsr[4] = {0u, 0u, 0u, 0u};
    STAMP(0, 0);
    const u64 epoch = load_epoch(ws);
    if (isY) {
      {
        const int row0 = (xcd * 32 + idx) * 16 + w * 4;
        u32x4 v[4];
        load_row4(v, x + (size_t)row0 * D_, lane);
        const float mu = row4_sum(v) * (1.0f / D_);
        const f32x2 m2 = {mu, mu};
        f32x2 q2 = {0.f, 0.f};
#pragma unroll
        for (int i = 0; i < 4; ++i)
#pragma unroll
          for (int e = 0; e < 4; ++e) { const f32x2 d = up2(v[i][e]) - m2; q2 = __builtin_elementwise_fma(d, d, q2); }
        const float rstd = __builtin_amdgcn_rsqf(dpp_add16(q2.x + q2.y) * (1.0f / D_) + 1e-5f);
        tg = next_tags(epoch);
        if (fr == 0) st16<ST_AUX>((u32x4){__float_as_uint(mu), tg.a, __float_as_uint(rstd), tg.b}, mk_rsrc(ws + WS_ROW, S_ * 16), (row0 + fq) * 16);
        if (w == 0 && lane == 0) { *(LAS u64*)(lds + T_EPO) = epoch; *(LAS unsigned*)(lds + T_EPO + 8) = 0u; }
      }
      asm volatile("s_waitcnt lgkmcnt(0)" ::: "memory");
      BAR();
      SCHED();
      STAMP(0, 1);
      p1_loop<0>(lds, acc, w, lane, [](int) {});
      STAMP(0, 2);
      BAR();
      MEMFENCE();
      {
        RowLN r;
        read_rowln(r, lds, wi, fq);
        const rsrc_t rO = mk_rsrc(ws + WS_CT + (size_t)c * 8 * 65536 + pm * 4096, 8 * 65536);
#pragma unroll
        for (int n = 0; n < 4; ++n) {
          const float c1 = *(LAS float*)(lds + T_COL + (wj * 64 + n * 16 + fr) * 4);
          const f32x2 cc = {c1, c1};
#pragma unroll
          for (int h = 0; h < 2; ++h) {
            const u32x4 o = {pk2(ln_val(r, acc, n, h, 0, cc)), pk2(ln_val(r, acc, n, h, 1, cc)), pk2(ln_val(r, acc, n, h, 2, cc)), pk2(ln_val(r, acc, n, h, 3, cc))};
            st16<ST_AUX>(o, rO, lane * 16 + (wj * 4 + n) * 65536 + (wi * 2 + h) * 1024);
          }
        }
      }
      asm volatile("s_waitcnt lgkmcnt(0)" ::: "memory");
      BAR();
      SCHED();
      STAMP(0, 3);
      p1_loop<1>(lds, acc, w, lane, [&](int k) { if (k == KPUB) publish_flag(ws, c, pm, w, lane, tg); });
      STAMP(0, 4);
      BAR();
      MEMFENCE();
      const rsrc_t rO = mk_rsrc(ws + WS_Z1 + (size_t)pm * 8 * 28 * 1024, c < 7 ? 8 * 28 * 1024 : 0);
#pragma unroll
      for (int h = 0; h < 2; ++h) {
        const f32x4 c0 = *(LAS f32x4*)(lds + T_COL + (wj * 64 + (h ^ wi) * 32 + 4 * fq) * 4);
        const f32x4 c1 = *(LAS f32x4*)(lds + T_COL + (wj * 64 + (h ^ wi) * 32 + 16 + 4 * fq) * 4);
#pragma unroll
        for (int m = 0; m < 4; ++m) {
          const f32x2 mr = *(LAS f32x2*)(lds + T_ROW + (wi * 64 + m * 16 + fr) * 8);
          const f32x2 nm = {-mr.x, -mr.x}, rs = {mr.y, mr.y};
          const f32x4 v0 = acc[m][2 * h], v1 = acc[m][2 * h + 1];
          u32x4 o;
          o.x = pk2(gelu2(rs * __builtin_elementwise_fma(nm, lo2(c0), lo2(v0))));
          o.y = pk2(gelu2(rs * __builtin_elementwise_fma(nm, hi2(c0), hi2(v0))));
          o.z = pk2(gelu2(rs * __builtin_elementwise_fma(nm, lo2(c1), lo2(v1))));
          o.w = pk2(gelu2(rs * __builtin_elementwise_fma(nm, hi2(c1), hi2(v1))));
          st16<ST_AUX>(o, rO, lane * 16 + ((wi * 4 + m) * 28 + zt * 4 + wj * 2 + (h ^ wi)) * 1024);
        }
      }
      STAMP(0, 5);
    } else {
#pragma unroll
      for (int n = 0; n < 4; ++n) bsr[n] = b_sp[pm * 128 + wj * 64 + n * 16 + fr];
      if (w == 0 && lane == 0) { *(LAS u64*)(lds + T_EPO) = epoch; *(LAS unsigned*)(lds + T_EPO + 8) = 0u; }
      asm volatile("s_waitcnt lgkmcnt(0)" : "+v"(bsr[0]), "+v"(bsr[1]), "+v"(bsr[2]), "+v"(bsr[3]) :: "memory");
      tg = next_tags(epoch);
      BAR();
      SCHED();
      STAMP(0, 1);
      p1_loop<2>(lds, acc, w, lane, [](int) {});
      STAMP(0, 2);
      BAR();
      MEMFENCE();
      f32x2 z[4][2][4];
      {
        RowLN r;
        read_rowln(r, lds, wi, fq);
        f32x2 s1[2][4], s2[2][4];
#pragma unroll
        for (int n = 0; n < 4; ++n) {
          const float c1 = *(LAS float*)(lds + T_COL + (wj * 64 + (n ^ (2 * wi)) * 16 + fr) * 4);
          const f32x2 cc = {c1, c1};
#pragma unroll
          for (int h = 0; h < 2; ++h)
#pragma unroll
            for (int e = 0; e < 4; ++e) {
              const f32x2 g = gelu2(ln_val(r, acc, n, h, e, cc));
              z[n][h][e] = g;
              if (n == 0) { s1[h][e] = g; s2[h][e] = g * g; } else { s1[h][e] += g; s2[h][e] = __builtin_elementwise_fma(g, g, s2[h][e]); }
            }
        }
#pragma unroll
        for (int h = 0; h < 2; ++h) {
          const float v[16] = {s1[h][0].x, s2[h][0].x, s1[h][0].y, s2[h][0].y, s1[h][1].x, s2[h][1].x, s1[h][1].y, s2[h][1].y,
                               s1[h][2].x, s2[h][2].x, s1[h][2].y, s2[h][2].y, s1[h][3].x, s2[h][3].x, s1[h][3].y, s2[h][3].y};
          *(LAS float*)(lds + T_PART + ((wj * 128 + wi * 64 + 32 * h + 8 * fq) * 2 + fr) * 4) = reduce_scatter16(v, fr);
        }
      }
      asm volatile("s_waitcnt lgkmcnt(0)" ::: "memory");
      BAR();
      MEMFENCE();
      if (w < 2) {
        const int row = w * 64 + lane;
        const f32x2 p = *(LAS f32x2*)(lds + T_PART + row * 8) + *(LAS f32x2*)(lds + T_PART + (128 + row) * 8);
        st16<ST_AUX>((u32x4){__float_as_uint(p.x), tg.a, __float_as_uint(p.y), tg.b}, mk_rsrc(ws + WS_STAT + (size_t)pm * 7 * 2048, 7 * 2048), row * 16 + pn * 2048);
      }
      STAMP(0, 3);
      BAR();
      MEMFENCE();
      STAMP(0, 4);
      const rsrc_t rO = mk_rsrc(ws + WS_CT + (size_t)c * 8 * 65536 + pm * 4096, 8 * 65536);
#pragma unroll
      for (int h = 0; h < 2; ++h) {
        f32x2 nm[4], rs[4];
#pragma unroll
        for (int e = 0; e < 4; ++e) {
          const int ro = (wi * 64 + 32 * h + 8 * fq + 2 * e) * 8;
          const f32x4 u = *(LAS f32x4*)(lds + T_FIN + ro) + *(LAS f32x4*)(lds + T_FIN + 1024 + ro);
          const f32x2 mu = (f32x2){u.x, u.z} * (1.0f / FH);
          const f32x2 var = (f32x2){u.y, u.w} * (1.0f / FH) - mu * mu;
          nm[e] = -mu;
          rs[e] = (f32x2){__builtin_amdgcn_rsqf(fmaxf(var.x, 0.f) + 1e-5f), __builtin_amdgcn_rsqf(fmaxf(var.y, 0.f) + 1e-5f)};
        }
#pragma unroll
        for (int n = 0; n < 4; ++n) {
          u32x4 o;
#pragma unroll
          for (int e = 0; e < 4; ++e) o[e] = pk2((z[n][h][e] + nm[e]) * rs[e]);
          st16<ST_AUX>(o, rO, lane * 16 + (wj * 4 + (n ^ (2 * wi))) * 65536 + (wi * 2 + h) * 1024);
        }
      }
      STAMP(0, 5);
      publish_flag(ws, c, pm, w, lane, tg);
      STAMP(0, 6);
    }
    p2_loop(lds, acc, w, lane, ws, [&](int it) { if (it == 0 && c < 7) publish_flag(ws, 15 + c, pm, w, lane, tg); });
    STAMP(1, 2);
    if (isY) {
      const rsrc_t rO = mk_rsrc(ws + WS_Y + ((size_t)type * 128 + pm * 8) * 16 * 1024, 8 * 16 * 1024);
#pragma unroll
      for (int n = 0; n < 4; ++n)
#pragma unroll
        for (int h = 0; h < 2; ++h) {
          const f32x4 v0 = acc[2 * h][n], v1 = acc[2 * h + 1][n];
          const u32x4 o = {pk2(lo2(v0)), pk2(hi2(v0)), pk2(lo2(v1)), pk2(hi2(v1))};
          st16<ST_AUX>(o, rO, lane * 16 + ((wj * 4 + n) * 16 + pn * 4 + wi * 2 + h) * 1024);
        }
    } else {
      const rsrc_t rO = mk_rsrc(ws + WS_H + (size_t)pm * 8 * 28 * 1024, 8 * 28 * 1024);
#pragma unroll
      for (int n = 0; n < 4; ++n) {
        const float b1 = bflo(bsr[n]);
        const f32x2 b = {b1, b1};
#pragma unroll
        for (int h = 0; h < 2; ++h) {
          const f32x4 v0 = acc[2 * h][n], v1 = acc[2 * h + 1][n];
          const u32x4 zz = *(LAS u32x4*)(lds + A_TAB + ((wj * 4 + n) * 4 + wi * 2 + h) * 1024 + lane * 16);
          u32x4 o;
          o.x = pk2(up2(zz.x) * (lo2(v0) + b));
          o.y = pk2(up2(zz.y) * (hi2(v0) + b));
          o.z = pk2(up2(zz.z) * (lo2(v1) + b));
          o.w = pk2(up2(zz.w) * (hi2(v1) + b));
          st16<ST_AUX>(o, rO, lane * 16 + ((wj * 4 + n) * 28 + pn * 4 + wi * 2 + h) * 1024);
        }
      }
    }
    STAMP(1, 5);
  }
  asm volatile("s_waitcnt lgkmcnt(0)" ::: "memory");
  BAR();
  MEMFENCE();
  if (!spare) publish_at(ws + WS_FL3 + (size_t)((pm * 2 + wj) * 32 + (isY ? 16 + c * 2 + wi : pn * 2 + wi)) * 8, lane, tg);
  STAMP(1, 6);
  {
    const unsigned lb = (unsigned)(size_t)lds;
    const int tt0 = pm * 8 + hb * 4;
    u32x2 xv[4];
#pragma unroll
    for (int n = 0; n < 4; ++n) xv[n] = *(const u32x2*)(x + (size_t)((tt0 + n) * 16 + fr) * D_ + on * 64 + w * 16 + 4 * fq);
    u32x2 wf[28][2];
    BAR();
    SCHED();
    {
      const int row = 4 * fq + (fr >> 2);
      const unsigned a0 = lb + row * 128 + ((w ^ ((row >> 1) & 3)) << 5) + (fr & 3) * 8, a1 = a0 + 57344;
      static_for<28>([&](auto S) {
        constexpr int s = S.value;
        dstr64<(s % 14) * 4096>(wf[s][0], s < 14 ? a0 : a1);
        dstr64<(s % 14) * 4096 + 2048>(wf[s][1], s < 14 ? a0 : a1);
        if constexpr ((s & 3) == 3) WAIT_L0();
      });
    }
    BAR();
    MEMFENCE();
    SCHED();
    u32x2 yr[4], yi[4];
    f32x4 oc[4];
#pragma unroll
    for (int n = 0; n < 4; ++n) oc[n] = (f32x4){0.f, 0.f, 0.f, 0.f};
    const unsigned h0 = lb + lane * 16, h1 = h0 + 65536;
    bf16x8 qa[8], qb[8];
    auto rdq = [&](bf16x8 (&q)[8], auto I, auto HC) {
      constexpr int i = I.value, hc = HC.value, ch = hc >> 1;
      ds128<(ch & 3) * 16384 + (i & 3) * 4096 + (2 * (hc & 1) + (i >> 2)) * 1024>(q[i], ch < 4 ? h0 : h1);
    };
    auto halfc = [&](const bf16x8 (&q)[8], bf16x8 (&g)[8], auto HC) {
      constexpr int hc = HC.value;
      static_for<8>([&](auto I) {
        constexpr int i = I.value, s = 2 * hc + (i >> 2);
        if constexpr (hc + 1 < 14) rdq(g, I, IC<hc + 1>{});
        asm volatile("s_waitcnt lgkmcnt(%0)" ::"n"(hc + 1 < 14 ? 8 : 7 - i) : "memory");
        SCHED();
        oc[i & 3] = MFMA(join(wf[s][0], wf[s][1]), q[i], oc[i & 3]);
        SCHED();
      });
    };
    SCHED();
    BAR();
    SCHED();
    STAMP(2, 1);
    static_for<8>([&](auto I) { rdq(qa, I, IC<0>{}); });
    static_for<7>([&](auto CH) {
      constexpr int ch = CH.value;
      halfc(qa, qb, IC<2 * ch>{});
      if constexpr (ch < 6) { BAR(); SCHED(); }
      if constexpr (ch == 2) {
        const rsrc_t rY = mk_rsrc(ws + WS_Y, 2 * 128 * 16 * 1024);
#pragma unroll
        for (int n = 0; n < 4; ++n) {
          yr[n] = __builtin_amdgcn_raw_buffer_load_b64(rY, lane * 16 + wj * 8, ((tt0 + n) * 16 + on * 2 + wi) * 1024, 16);
          yi[n] = __builtin_amdgcn_raw_buffer_load_b64(rY, lane * 16 + wj * 8, ((128 + tt0 + n) * 16 + on * 2 + wi) * 1024, 16);
        }
        SCHED();
      }
      halfc(qb, qa, IC<2 * ch + 1>{});
    });
    STAMP(2, 2);
#pragma unroll
    for (int n = 0; n < 4; ++n) {
      u32x2 o;
      o.x = pk2(up2(xv[n].x) + (up2(yr[n].x) - up2(yi[n].x)) + lo2(oc[n]));
      o.y = pk2(up2(xv[n].y) + (up2(yr[n].y) - up2(yi[n].y)) + hi2(oc[n]));
      *(u32x2*)(out + (size_t)((tt0 + n) * 16 + fr) * D_ + on * 64 + w * 16 + 4 * fq) = o;
    }
    STAMP(2, 5);
    STAMP(2, 6);
    asm volatile(".Lfm_e2:");
  }
#endif
}

extern "C" void kernel_launch(void* const* d_in, const int* in_sizes, int n_in, void* d_out, int out_size, void* d_ws,
                              size_t ws_size, hipStream_t stream) {
  const u16* x = (const u16*)d_in[0];
  const u16* ws_re = (const u16*)d_in[1];
  const u16* ws_im = (const u16*)d_in[2];
  const u16* wd_re = (const u16*)d_in[3];
  const u16* wd_im = (const u16*)d_in[4];
  const u16* w_in = (const u16*)d_in[6];
  const u16* w_sp = (const u16*)d_in[7];
  const u16* b_sp = (const u16*)d_in[8];
  const u16* w_out = (const u16*)d_in[9];
  (void)hipFuncSetAttribute((const void*)fm_main, hipFuncAttributeMaxDynamicSharedMemorySize, A_LDS);
  fm_main<<<256, 512, A_LDS, stream>>>(x, wd_re, wd_im, w_in, ws_re, ws_im, w_sp, b_sp, w_out, (char*)d_ws, (u16*)d_out);
}
```

```cpp
#include <hip/hip_runtime.h>
#include <hip/hip_bf16.h>
#include <stdint.h>

typedef unsigned short u16;
typedef __bf16 bf16x8 __attribute__((ext_vector_type(8)));
typedef float f32x4 __attribute__((ext_vector_type(4)));
typedef float f32x2 __attribute__((ext_vector_type(2)));
typedef unsigned u32x4 __attribute__((ext_vector_type(4)));
typedef unsigned u32x2 __attribute__((ext_vector_type(2)));
typedef unsigned long long u64;
#define LAS __attribute__((address_space(3)))
#define GAS __attribute__((address_space(1)))
typedef __amdgpu_buffer_rsrc_t rsrc_t;

constexpr int S_ = 2048, D_ = 512, F_ = 1792, FH = 896;

constexpr size_t WS_EPOCH = 0;
constexpr size_t WS_STAT = 4096;
constexpr size_t WS_ROW = 512u << 10;
constexpr size_t WS_COL = 576u << 10;
constexpr size_t WS_FLG = 640u << 10;
constexpr size_t WS_FL3 = 672u << 10;
constexpr size_t WS_CT = 1u << 20;
constexpr size_t WS_Z1 = WS_CT + (size_t)120 * 65536;
constexpr size_t WS_H = WS_Z1 + (size_t)128 * 28 * 1024;
constexpr size_t WS_Y = WS_H + (size_t)128 * 28 * 1024;

#define ST_AUX 16

#define STAMP(kern, i) do {} while (0)
#define STEPSTAMP(k) do {} while (0)
template <int V> struct IC { static constexpr int value = V; };
template <int N, int I = 0, class F> __device__ __forceinline__ void static_for(F&& f) {
  if constexpr (I < N) { f(IC<I>{}); static_for<N, I + 1>(f); }
}

__device__ __forceinline__ float bflo(unsigned w) { return __uint_as_float(w << 16); }
__device__ __forceinline__ float bfhi(unsigned w) { return __uint_as_float(w & 0xffff0000u); }
__device__ __forceinline__ unsigned pk_bf16(float lo, float hi) {
  typedef __bf16 bf16x2 __attribute__((ext_vector_type(2)));
  f32x2 v = {lo, hi};
  return __builtin_bit_cast(unsigned, __builtin_convertvector(v, bf16x2));
}
__device__ __forceinline__ float gelu_tanh(float x) {
  const float c1 = -2.0f * 0.7978845608028654f * 1.4426950408889634f;
  const float c3 = c1 * 0.044715f;
  const float t = x * __builtin_fmaf(x * x, c3, c1);
  return x * __builtin_amdgcn_rcpf(1.0f + __builtin_amdgcn_exp2f(t));
}
__device__ __forceinline__ f32x2 gelu2(f32x2 x) {
  const float c1 = -2.0f * 0.7978845608028654f * 1.4426950408889634f;
  const float c3 = c1 * 0.044715f;
  const f32x2 t = x * __builtin_elementwise_fma(x * x, (f32x2){c3, c3}, (f32x2){c1, c1});
  f32x2 e = {__builtin_amdgcn_exp2f(t.x), __builtin_amdgcn_exp2f(t.y)};
  e = e + (f32x2){1.0f, 1.0f};
  return x * (f32x2){__builtin_amdgcn_rcpf(e.x), __builtin_amdgcn_rcpf(e.y)};
}
__device__ __forceinline__ f32x2 lo2(f32x4 v) { return (f32x2){v.x, v.y}; }
__device__ __forceinline__ f32x2 hi2(f32x4 v) { return (f32x2){v.z, v.w}; }
__device__ __forceinline__ unsigned pk2(f32x2 v) { return pk_bf16(v.x, v.y); }
__device__ __forceinline__ f32x2 up2(unsigned w) { return (f32x2){bflo(w), bfhi(w)}; }
__device__ __forceinline__ rsrc_t mk_rsrc(const void* p, unsigned bytes) {
  return __builtin_amdgcn_make_buffer_rsrc((void*)p, (short)0, (int)bytes, 0x00020000);
}
#define WAIT_V(n) asm volatile("s_waitcnt vmcnt(%0)" ::"n"(n) : "memory")
#define BAR() __builtin_amdgcn_s_barrier()
#define SCHED() __builtin_amdgcn_sched_barrier(0)
#define MEMFENCE() asm volatile("" ::: "memory")
#define WAIT_L0() do { asm volatile("s_waitcnt lgkmcnt(0)" ::: "memory"); SCHED(); } while (0)
#define DMA16(rs, ldsp, voff, soff) __builtin_amdgcn_raw_ptr_buffer_load_lds(rs, (LAS void*)(ldsp), 16, voff, soff, 0, 0)
#define DMA16D(rs, ldsp, voff, soff) __builtin_amdgcn_raw_ptr_buffer_load_lds(rs, (LAS void*)(ldsp), 16, voff, soff, 0, 16)
#define TOUCH(rs, ldsp, voff, soff) __builtin_amdgcn_raw_ptr_buffer_load_lds(rs, (LAS void*)(ldsp), 4, voff, soff, 0, 0)
#define MFMA(a, b, c) __builtin_amdgcn_mfma_f32_16x16x32_bf16(a, b, c, 0, 0, 0)

template <int AUX> __device__ __forceinline__ void st16(u32x4 v, rsrc_t r, int off) { __builtin_amdgcn_raw_buffer_store_b128(v, r, off, 0, AUX); }
template <int OFF> __device__ __forceinline__ void ds128(bf16x8& d, unsigned a) {
  asm volatile("ds_read_b128 %0, %1 offset:%2" : "=v"(d) : "v"(a), "n"(OFF));
}
template <int OFF> __device__ __forceinline__ void dstr64(u32x2& d, unsigned a) {
  asm volatile("ds_read_b64_tr_b16 %0, %1 offset:%2" : "=v"(d) : "v"(a), "n"(OFF));
}
__device__ __forceinline__ bf16x8 join(const u32x2& lo, const u32x2& hi) {
  return __builtin_bit_cast(bf16x8, (u32x4){lo.x, lo.y, hi.x, hi.y});
}
__device__ __forceinline__ float dpp_add16(float v) {
  v += __builtin_bit_cast(float, __builtin_amdgcn_update_dpp(0, __builtin_bit_cast(int, v), 0x128, 0xf, 0xf, false));
  v += __builtin_bit_cast(float, __builtin_amdgcn_update_dpp(0, __builtin_bit_cast(int, v), 0x124, 0xf, 0xf, false));
  v += __builtin_bit_cast(float, __builtin_amdgcn_update_dpp(0, __builtin_bit_cast(int, v), 0x122, 0xf, 0xf, false));
  v += __builtin_bit_cast(float, __builtin_amdgcn_update_dpp(0, __builtin_bit_cast(int, v), 0x121, 0xf, 0xf, false));
  return v;
}

template <int CTRL> __device__ __forceinline__ float dpp_mov(float v) {
  return __builtin_bit_cast(float, __builtin_amdgcn_update_dpp(0, __builtin_bit_cast(int, v), CTRL, 0xf, 0xf, false));
}
__device__ __forceinline__ float reduce_scatter16(const float (&v)[16], int fr) {
  float a[8], b[4], c[2];
  const bool b3 = fr & 8, b2 = fr & 4, b1 = fr & 2, b0 = fr & 1;
#pragma unroll
  for (int i = 0; i < 8; ++i) a[i] = (b3 ? v[i + 8] : v[i]) + dpp_mov<0x128>(b3 ? v[i] : v[i + 8]);
#pragma unroll
  for (int i = 0; i < 4; ++i) b[i] = (b2 ? a[i + 4] : a[i]) + dpp_mov<0x141>(b2 ? a[i] : a[i + 4]);
#pragma unroll
  for (int i = 0; i < 2; ++i) c[i] = (b1 ? b[i + 2] : b[i]) + dpp_mov<0x4E>(b1 ? b[i] : b[i + 2]);
  return (b0 ? c[1] : c[0]) + dpp_mov<0xB1>(b0 ? c[0] : c[1]);
}
__device__ __forceinline__ float wave_sum(float s) { s = dpp_add16(s); s += __shfl_xor(s, 16); s += __shfl_xor(s, 32); return s; }

constexpr int A_TAB = 131072;
constexpr int T_ROW = A_TAB, T_COL = A_TAB + 1024, T_PART = A_TAB + 2048, T_FIN = A_TAB + 4096, T_EPO = A_TAB + 6144, T_JUNK = A_TAB + 7168;
constexpr int A_LDS = A_TAB + 32768;
constexpr int SPIN = 1 << 16;
#define KMID 4
#define KPUB 2
#define SPQ 64
#define SDELAY 16
#define FDELAY 24
#define PSLEEP 1
#define QDELAY 12

struct Tags { unsigned a, b; };
__device__ __forceinline__ Tags next_tags(u64 e) { return Tags{(unsigned)e + 1u, (unsigned)(e >> 32) + 0x9E3779B9u}; }
__device__ __forceinline__ u64 tag64(Tags t) { return ((u64)t.b << 32) | t.a; }
__device__ __forceinline__ u64 load_epoch(char* ws) { return __hip_atomic_load((GAS u64*)(ws + WS_EPOCH), __ATOMIC_RELAXED, __HIP_MEMORY_SCOPE_AGENT); }
__device__ __forceinline__ void ld16_issue(u32x4& g, const char* p) { asm volatile("global_load_dwordx4 %0, %1, off sc1" : "=&v"(g) : "v"(p) : "memory"); }
__device__ __forceinline__ u32x4 ld16_now(const char* p) {
  u32x4 g;
  asm volatile("global_load_dwordx4 %0, %1, off sc1\n\ts_waitcnt vmcnt(0)" : "=&v"(g) : "v"(p) : "memory");
  return g;
}
__device__ __forceinline__ u32x2 ld8_now(const char* p) {
  u32x2 g;
  asm volatile("global_load_dwordx2 %0, %1, off sc1\n\ts_waitcnt vmcnt(0)" : "=&v"(g) : "v"(p) : "memory");
  return g;
}
__device__ __forceinline__ int korder32(int t, int j, int vl) {
  if (t < 1) return j;
  const int t1 = t - 1;
  if (t1 < 28) { const int u = t1 >> 2, v = t1 & 3; return v == vl ? 4 * (u + 1) + j : 4 * u + ((j + 1 + (v < vl ? v : v - 1)) & 3); }
  return 28 + ((j + 1 + (t1 - 28)) & 3);
}
__device__ __forceinline__ void load_row4(u32x4 (&v)[4], const u16* rows, int lane) {
#pragma unroll
  for (int i = 0; i < 4; ++i) v[i] = *(const u32x4*)(rows + (size_t)(lane >> 4) * D_ + i * 128 + (lane & 15) * 8);
}
__device__ __forceinline__ float row4_sum(const u32x4 (&v)[4]) {
  f32x2 s = {0.f, 0.f};
#pragma unroll
  for (int i = 0; i < 4; ++i) s += (up2(v[i].x) + up2(v[i].y)) + (up2(v[i].z) + up2(v[i].w));
  return dpp_add16(s.x + s.y);
}

template <int MODE, class Hook>
__device__ __forceinline__ void p1_loop(LAS char* lds, f32x4 (&acc)[4][4], int w, int lane, Hook&& hook) {
  const int wi = w >> 1, wj = w & 1, fr = lane & 15, fq = lane >> 4;
  const unsigned lb = (unsigned)(size_t)lds;
  constexpr int NC = MODE == 0 ? 0 : 2, NAW = MODE == 0 ? 2 : 4;
  unsigned aX[2], aW[NAW];
#pragma unroll
  for (int ks = 0; ks < 2; ++ks) {
    aX[ks] = lb + (((wi * 64 + fr) * 128 + ((fq ^ (fr >> 1)) << 4)) ^ (ks * 64));
    if constexpr (MODE == 0) aW[ks] = lb + 16384 + (((wj * 64 + fr) * 128 + ((fq ^ (fr >> 1)) << 4)) ^ (ks * 64));
  }
  if constexpr (MODE != 0) {
    const int tq = fr >> 2, tp = fr & 3, tf = ((tq >> 1) & 1) | ((fq & 1) << 1);
#pragma unroll
    for (int n = 0; n < 4; ++n) aW[n] = lb + 16384 + wj * 8192 + (8 * fq + tq) * 128 + ((n ^ (2 * wi) ^ tf) << 5) + tp * 8;
  }
  f32x4 cs[2];
#pragma unroll
  for (int m = 0; m < 4; ++m)
#pragma unroll
    for (int n = 0; n < 4; ++n) acc[m][n] = (f32x4){0.f, 0.f, 0.f, 0.f};
  cs[0] = (f32x4){0.f, 0.f, 0.f, 0.f}; cs[1] = cs[0];
  bf16x8 ones = __builtin_bit_cast(bf16x8, (u32x4){0x3f803f80u, 0x3f803f80u, 0x3f803f80u, 0x3f803f80u});
  asm volatile("" : "+v"(ones));
  struct Frag { bf16x8 x[4]; bf16x8 w[MODE == 0 ? 4 : 1]; u32x2 t[MODE == 0 ? 1 : 4][2]; };
  Frag fa, fb;
  constexpr int NR = MODE == 0 ? 8 : 12, NM = 16 + NC;
  auto rd = [&](Frag& f, auto I, auto KS) {
    constexpr int i = I.value, ks = KS.value;
    if constexpr (i < 4) ds128<i * 2048>(f.x[i], aX[ks]);
    else if constexpr (MODE == 0) ds128<(i - 4) * 2048>(f.w[i - 4], aW[ks]);
    else { constexpr int n = (i - 4) >> 1, t = (i - 4) & 1; dstr64<(32 * ks + 4 * t) * 128>(f.t[n][t], aW[n]); }
  };
  auto wfrag = [&](const Frag& f, int n) -> bf16x8 { if constexpr (MODE == 0) return f.w[n]; else return join(f.t[n][0], f.t[n][1]); };
  int dS = 32768;
  auto half = [&](const Frag& f, Frag& g, auto KS) {
    static_for<NM>([&](auto I) {
      constexpr int i = I.value;
      if constexpr (i < 16) {
        constexpr int n = i >> 2, m = i & 3;
        if constexpr (MODE == 1) acc[m][n] = MFMA(wfrag(f, n), f.x[m], acc[m][n]);
        else acc[m][n] = MFMA(f.x[m], wfrag(f, n), acc[m][n]);
      } else cs[i - 16] = MFMA(ones, wfrag(f, i - 16), cs[i - 16]);
      SCHED();
      if constexpr (i < NR) { rd(g, I, KS); SCHED(); }
      else if constexpr (KS.value == 1 && i - NR < 2 + NAW) {
        if constexpr (i - NR < 2) aX[i - NR] += dS; else aW[i - NR - 2] += dS;
        SCHED();
      }
    });
    WAIT_L0();
  };
  static_for<NR>([&](auto I) { rd(fa, I, IC<0>{}); });
  WAIT_L0();
#pragma unroll 1
  for (int k = 0; k < 8; ++k) {
    dS = (k & 3) == 3 ? -98304 : 32768;
    half(fa, fb, IC<1>{});
    BAR();
    SCHED();
    half(fb, fa, IC<0>{});
    hook(k);
    SCHED();
  }
  if constexpr (NC > 0) {
    LAS float* colt = (LAS float*)(lds + T_COL);
    if (fq == 0) { colt[wj * 64 + wi * 32 + fr] = cs[0][0]; colt[wj * 64 + wi * 32 + 16 + fr] = cs[1][0]; }
  }
  asm volatile("s_waitcnt lgkmcnt(0)" ::: "memory");
}

template <class Hook>
__device__ __forceinline__ void p2_loop(LAS char* lds, f32x4 (&acc)[4][4], int w, int lane, char* ws, Hook&& hook) {
  const int wi = w >> 1, wj = w & 1, fr = lane & 15, fq = lane >> 4;
  const unsigned lb = (unsigned)(size_t)lds;
  unsigned aP[2], aQ[2][2];
#pragma unroll
  for (int sp = 0; sp < 2; ++sp) {
    aP[sp] = lb + sp * 65536 + wi * 8192 + lane * 16;
#pragma unroll
    for (int ks = 0; ks < 2; ++ks) aQ[sp][ks] = lb + sp * 65536 + 16384 + (((wj * 64 + fr) * 128 + ((fq ^ (fr >> 1)) << 4)) ^ (ks * 64));
  }
#pragma unroll
  for (int m = 0; m < 4; ++m)
#pragma unroll
    for (int n = 0; n < 4; ++n) acc[m][n] = (f32x4){0.f, 0.f, 0.f, 0.f};
  bf16x8 fa[8], fb[8];
  auto rd = [&](bf16x8 (&f)[8], auto I, auto S, auto KS) {
    constexpr int x = I.value, s = S.value, ks = KS.value;
    if constexpr (x < 4) ds128<(s & 1) * 32768 + (x * 2 + ks) * 1024>(f[x], aP[s >> 1]);
    else ds128<(s & 1) * 32768 + (x - 4) * 2048>(f[x], aQ[s >> 1][ks]);
  };
  auto half = [&](const bf16x8 (&f)[8], bf16x8 (&g)[8], auto S, auto KS) {
    static_for<16>([&](auto I) {
      constexpr int i = I.value;
      acc[i >> 2][i & 3] = MFMA(f[i >> 2], f[4 + (i & 3)], acc[i >> 2][i & 3]);
      SCHED();
      if constexpr (i < 8) { rd(g, I, S, KS); SCHED(); }
    });
    WAIT_L0();
  };
  BAR();
  SCHED();
  STAMP(1, 1);
  static_for<8>([&](auto I) { rd(fa, I, IC<0>{}, IC<0>{}); });
  WAIT_L0();
#pragma unroll 1
  for (int it = 0; it < 8; ++it) {
    static_for<4>([&](auto S) {
      half(fa, fb, S, IC<1>{});
      BAR();
      SCHED();
      half(fb, fa, IC<(S.value + 1) & 3>{}, IC<0>{});
    });
    hook(it);
    SCHED();
  }
}

struct RowLN { f32x2 nm[2][4], rs[2][4]; };
__device__ __forceinline__ void read_rowln(RowLN& r, LAS char* lds, int wi, int fq) {
#pragma unroll
  for (int h = 0; h < 2; ++h)
#pragma unroll
    for (int e = 0; e < 4; ++e) {
      const f32x4 t = *(LAS f32x4*)(lds + T_ROW + (wi * 64 + 32 * h + 8 * fq + 2 * e) * 8);
      r.nm[h][e] = (f32x2){-t.x, -t.z}; r.rs[h][e] = (f32x2){t.y, t.w};
    }
}
__device__ __forceinline__ f32x2 ln_val(const RowLN& r, const f32x4 (&acc)[4][4], int n, int h, int e, f32x2 c) {
  const f32x4 v = acc[2 * h + (e >> 1)][n];
  return r.rs[h][e] * __builtin_elementwise_fma(r.nm[h][e], c, (e & 1) ? hi2(v) : lo2(v));
}
__device__ __forceinline__ void publish_at(char* p, int lane, Tags tg) {
  WAIT_V(0);
  if (lane == 0) __hip_atomic_store((GAS u64*)p, tag64(tg), __ATOMIC_RELAXED, __HIP_MEMORY_SCOPE_AGENT);
}
__device__ __forceinline__ void publish_flag(char* ws, int c, int pm, int w, int lane, Tags tg) {
  publish_at(ws + WS_FLG + (size_t)(c * 64 + pm * 4 + w) * 8, lane, tg);
}
__device__ __forceinline__ void wait_flags(const char* fp, Tags tg) {
#pragma unroll 1
  for (int spin = 0; spin < SPIN; ++spin) {
    const u32x2 fl = ld8_now(fp);
    if (__all(fl.x == tg.a && fl.y == tg.b)) break;
    __builtin_amdgcn_s_sleep(PSLEEP);
  }
}

struct Ring2 {
  rsrc_t rQ, rP; unsigned voffQ, voffP; LAS char* lw_lds; int j, vl;
  __device__ __forceinline__ void Q(int buf, int kk) const {
#pragma unroll
    for (int i = 0; i < 4; ++i) DMA16(rQ, lw_lds + buf * 32768 + 16384 + i * 4096, voffQ, kk * 128 + i * 32 * S_ * 2);
  }
  __device__ __forceinline__ void P(int buf, int kk) const {
#pragma unroll
    for (int i = 0; i < 4; ++i) DMA16D(rP, lw_lds + buf * 32768 + i * 4096, voffP, kk * 2048 + i * 131072);
  }
  __device__ __forceinline__ int ord(int t) const { return korder32(t, j, vl); }
};

template <bool G>
__device__ __forceinline__ Tags p1_loader(LAS char* lds, char* ws, const u16* x, const u16* wd, const u16* win, const Ring2& r2,
                                          int lm, int pm, int pn, int c, int lw, int lane) {
  constexpr int NK = G ? 8 : 16;
  const int rho = 8 * lw + (lane >> 3);
  const unsigned sw = (unsigned)(((lane & 7) ^ ((rho >> 1) & 7)) << 4);
  const rsrc_t rX = mk_rsrc(x + (size_t)pm * 128 * D_, 128 * D_ * 2);
  const unsigned voffXp = (unsigned)((8 * ((rho >> 2) & 3) + 4 * (rho >> 4) + (rho & 3)) * (D_ * 2)) + sw;
  const unsigned voffXn = (unsigned)(rho * (D_ * 2)) + sw;
  const rsrc_t rW = mk_rsrc(win, (D_ - 1) * F_ * 2 + 256), rD = mk_rsrc(wd, 128 * D_ * 2);
  const int f = ((rho >> 1) & 1) | (((rho >> 3) & 1) << 1);
  const unsigned voffW = (unsigned)(rho * (F_ * 2) + (((((lane & 7) >> 1) ^ f) << 5) | ((lane & 1) << 4)));
  const int k0 = G ? (2 * lm + pn) & 7 : (lm + 2 * pn) & 7;
  LAS char* lw_lds = lds + lw * 1024;
  auto p1part = [&](auto H) {
    constexpr int h = H.value, t = h >> 1;
    const int kk = (t + k0) & 7;
    LAS char* p = lw_lds + (t & 3) * 32768;
    if constexpr ((h & 1) == 0) {
#pragma unroll
      for (int i = 0; i < 4; ++i) DMA16(rX, p + i * 4096, t < 8 ? voffXp : voffXn, kk * 128 + i * 32 * D_ * 2);
    } else if constexpr (!G && t < 8) {
#pragma unroll
      for (int i = 0; i < 4; ++i) DMA16(rD, p + 16384 + i * 4096, voffXn, kk * 128 + i * 32 * D_ * 2);
    } else {
#pragma unroll
      for (int i = 0; i < 4; ++i) DMA16(rW, p + 16384 + i * 4096, voffW, (kk * 64 + (i & 1) * 32) * (F_ * 2) + (i >> 1) * 128);
    }
  };
  constexpr int BG = G ? 6 : 5;
  constexpr auto upto = [](int b) constexpr { int h = 3 + 3 * b, lim = b == 0 ? 8 : 2 * (b + 3); h = h < lim ? h : lim; return h < 2 * NK ? h : 2 * NK; };
  constexpr auto after = [upto](int hx, int b) constexpr {
    int n = 0;
    for (int s = 0; s <= b; ++s) {
      for (int h = s ? upto(s - 1) : 0; h < upto(s); ++h) n = h == hx ? 0 : n + 4;
      n += s == BG ? 1 : 0;
      if (!G && s >= 14) n += 4;
    }
    return n;
  };
  const int row = (lw & 1) * 64 + lane, hf = lw >> 1;
  const char* gp = ws + (G || lw < 2 ? WS_ROW + (size_t)(pm * 128 + row) * 16 : WS_COL + (size_t)(c * 64 + lane) * 16);
  u32x4 g;
  Tags tg = {0u, 0u};
  auto table1 = [&]() {
    asm volatile("" : "+v"(g));
    tg = next_tags(*(LAS u64*)(lds + T_EPO));
    if (lw < (G ? 2 : 3)) {
#pragma unroll 1
      for (int spin = 0; spin < SPIN; ++spin) {
        if (__all(g.y == tg.a && g.w == tg.b)) break;
        __builtin_amdgcn_s_sleep(2);
        g = ld16_now(gp);
      }
      *(LAS f32x2*)(lds + (lw < 2 ? T_ROW + row * 8 : T_COL + lane * 8)) = (f32x2){__uint_as_float(g.x), __uint_as_float(g.z)};
      asm volatile("s_waitcnt lgkmcnt(0)" ::: "memory");
    }
  };
  static_for<NK>([&](auto B) {
    constexpr int b = B.value, h0 = b ? upto(b - 1) : 0, h1 = upto(b);
    static_for<h1 - h0>([&](auto H) { p1part(IC<h0 + H.value>{}); });
    if constexpr (b == BG) ld16_issue(g, gp);
    if constexpr (!G && b == 9) {
      WAIT_V(32);
      table1();
      BAR();
      WAIT_V(after(17, 9));
      BAR();
    }
    if constexpr (!G && b >= 14) r2.Q(b - 14, r2.ord(b - 14));
    if constexpr (!(!G && b == 8)) WAIT_V(after(2 * b + 1, b));
    BAR();
    MEMFENCE();
  });
  if constexpr (G) {
    BAR();
    WAIT_V(0);
    table1();
    BAR();
    r2.Q(0, r2.ord(0)); r2.Q(1, r2.ord(1)); r2.Q(2, r2.ord(2)); r2.Q(3, r2.ord(3));
    BAR();
    const char* sp = ws + WS_STAT + (size_t)pm * 7 * 2048 + hf * 3 * 2048 + row * 16;
    u32x4 q[4];
    __builtin_amdgcn_s_sleep(SDELAY);
#pragma unroll 1
    for (int spin = 0; spin < SPIN; ++spin) {
#pragma unroll
      for (int i = 0; i < 4; ++i) ld16_issue(q[i], sp + i * 2048);
      asm volatile("s_waitcnt vmcnt(0)" : "+v"(q[0]), "+v"(q[1]), "+v"(q[2]), "+v"(q[3]) :: "memory");
      bool ok = true;
#pragma unroll
      for (int i = 0; i < 4; ++i) ok = ok && q[i].y == tg.a && q[i].w == tg.b;
      if (__all(ok)) break;
    }
    const float t1 = __uint_as_float(q[1].x) + __uint_as_float(q[2].x) + __uint_as_float(q[3].x) + (hf ? 0.f : __uint_as_float(q[0].x));
    const float t2 = __uint_as_float(q[1].z) + __uint_as_float(q[2].z) + __uint_as_float(q[3].z) + (hf ? 0.f : __uint_as_float(q[0].z));
    *(LAS f32x2*)(lds + T_FIN + (hf * 128 + row) * 8) = (f32x2){t1, t2};
    asm volatile("s_waitcnt lgkmcnt(0)" ::: "memory");
    BAR();
    __builtin_amdgcn_s_sleep(FDELAY);
  } else {
    r2.Q(2, r2.ord(2));
    BAR();
    r2.Q(3, r2.ord(3));
    BAR();
  }
  return tg;
}

__global__ __launch_bounds__(512, 2) void fm_main(const u16* __restrict__ x, const u16* __restrict__ wd_re,
                                                  const u16* __restrict__ wd_im, const u16* __restrict__ w_in,
                                                  const u16* __restrict__ ws_re, const u16* __restrict__ ws_im,
                                                  const u16* __restrict__ w_sp, const u16* __restrict__ b_sp,
                                                  const u16* __restrict__ w_out, char* ws, u16* __restrict__ out) {
#if defined(__HIP_DEVICE_COMPILE__)
  extern __shared__ __attribute__((aligned(16))) char smem[];
  LAS char* lds = (LAS char*)smem;
  const int w = __builtin_amdgcn_readfirstlane(threadIdx.x >> 6), lane = threadIdx.x & 63;
  const int xcd = blockIdx.x & 7, idx = blockIdx.x >> 3;
  const int wi = (w >> 1) & 1, wj = w & 1, fr = lane & 15, fq = lane >> 4;
  const bool isY = xcd < 4, spare = !isY && idx >= 28;
  const int type = xcd >> 1;
  const int lm = isY ? idx >> 2 : spare ? idx - 28 : idx / 7;
  const int pm = isY ? (xcd & 1) * 8 + lm : (xcd - 4) * 4 + lm;
  const int pn = isY ? idx & 3 : spare ? 7 : idx % 7;
  const int c = isY ? type * 4 + pn : 8 + pn;
  const int zt = !isY ? pn : c < 7 ? c : 6;
  const int hb = isY ? 1 : 0, on = isY ? c : pn;
  {
    u64 pc; int len1, len2;
    asm volatile("s_getpc_b64 %0\n.Lfm_pc:\n\ts_mov_b32 %1, .Lfm_e1-.Lfm_pc\n\ts_mov_b32 %2, .Lfm_e2-.Lfm_pc" : "=s"(pc), "=s"(len1), "=s"(len2));
    const unsigned len = (unsigned)(len1 > len2 ? len1 : len2);
    if (w == 3 && len < 65536u && (unsigned)idx * 8192u < len) TOUCH(mk_rsrc((const void*)pc, len), lds + T_JUNK, lane * 128, idx * 8192);
  }
  Tags tg = {0u, 0u};
  if (spare) {
    const int sid = pm;
    tg = next_tags(load_epoch(ws));
    const int r0 = sid * 64 + w * 8;
    const u16* src = (r0 < 512 ? wd_re : wd_im) + (size_t)(r0 & 511) * D_;
    u32x4 va[4], vb[4];
#pragma unroll
    for (int i = 0; i < 4; ++i) {
      va[i] = *(const u32x4*)(src + (size_t)(2 * fq) * D_ + i * 128 + fr * 8);
      vb[i] = *(const u32x4*)(src + (size_t)(2 * fq + 1) * D_ + i * 128 + fr * 8);
    }
    const float sa = row4_sum(va), sb = row4_sum(vb);
    if (fr == 0) st16<ST_AUX>((u32x4){__float_as_uint(sa), tg.a, __float_as_uint(sb), tg.b}, mk_rsrc(ws + WS_COL, 512 * 16), (r0 / 2 + fq) * 16);
    MEMFENCE();
    BAR();
    MEMFENCE();
    {
      const rsrc_t rA = mk_rsrc(w_in, D_ * F_ * 2), rB = mk_rsrc(w_sp + (size_t)(xcd - 4) * 512 * S_, 512 * S_ * 2), rC = mk_rsrc(w_out, FH * D_ * 2);
      if (w < 7) {
        const int id = (sid * 7 + w) * 64 + lane;
        TOUCH(rA, lds + T_JUNK, (id / 14) * (F_ * 2) + (id % 14) * 128, 0);
      }
#pragma unroll 1
      for (int ch = w; ch < SPQ; ch += 8) {
        const int id = (ch * 4 + lm) * 64 + lane;
        TOUCH(rB, lds + T_JUNK, ((id >> 1) & 511) * (S_ * 2) + (2 * (id >> 10) + (id & 1)) * 128, 0);
        if (ch == w + 16 && w < 7) TOUCH(rC, lds + T_JUNK, lane * 128, (sid * 7 + w) * 8192);
      }
    }
    if (sid == 0 && w == 0) {
#pragma unroll 1
      for (int spin = 0; spin < SPIN; ++spin) {
        u32x4 g[16];
#pragma unroll
        for (int i = 0; i < 16; ++i)
          ld16_issue(g[i], i < 8 ? ws + WS_COL + (size_t)(i * 64 + lane) * 16 : ws + WS_FLG + (size_t)(i < 15 ? (i - 8) * 64 + lane : 7 * 64 + (lane & 31)) * 16);
        WAIT_V(0);
        bool ok = true;
#pragma unroll
        for (int i = 0; i < 16; ++i) {
          asm volatile("" : "+v"(g[i]));
          ok = ok && (i < 8 ? (g[i].y == tg.a && g[i].w == tg.b) : (g[i].x == tg.a && g[i].y == tg.b && g[i].z == tg.a && g[i].w == tg.b));
        }
        if (__all(ok)) break;
        __builtin_amdgcn_s_sleep(32);
      }
      if (lane == 0) __hip_atomic_store((GAS u64*)(ws + WS_EPOCH), tag64(tg), __ATOMIC_RELAXED, __HIP_MEMORY_SCOPE_AGENT);
    }
    WAIT_V(0);
  }

  if (w >= 4) {
    const int lw = w - 4, rho = 8 * lw + (lane >> 3);
    LAS char* lw_lds = lds + lw * 1024;
    const rsrc_t rW = mk_rsrc(w_out + on * 64, (FH - 1) * D_ * 2 + 128);
    const unsigned voffW = (unsigned)(rho * (D_ * 2) + (((((lane & 7) >> 1) ^ ((rho >> 1) & 3)) << 5) | ((lane & 1) << 4)));
    const int k3 = on < 7 ? on : 0;
    auto ord3 = [&](int t) { return t + k3 >= 7 ? t + k3 - 7 : t + k3; };
    auto issW = [&](int t0, int t1) {
#pragma unroll 1
      for (int t = t0; t < t1; ++t) {
        const int kk = ord3(t);
#pragma unroll
        for (int i = 0; i < 4; ++i) DMA16(rW, lw_lds + t * 16384 + i * 4096, voffW, (kk * 128 + i * 32) * (D_ * 2));
      }
    };
    if (!spare) {
      Ring2 r2;
      r2.rQ = mk_rsrc((isY ? (type ? ws_im : ws_re) : w_sp) + (size_t)pm * 128 * S_, 128 * S_ * 2);
      r2.rP = mk_rsrc(ws + WS_CT + (size_t)c * 8 * 65536, 8 * 65536);
      r2.voffQ = (unsigned)(rho * (S_ * 2) + (((lane & 7) ^ ((rho >> 1) & 7)) << 4));
      r2.voffP = (unsigned)((lw >> 1) * 65536 + (lw & 1) * 1024 + lane * 16);
      r2.lw_lds = lw_lds;
      r2.j = (lm + pn) & 3;
      r2.vl = (isY ? (lm >> 2) * 2 + (pn & 1) : (pn >> 2) * 2 + (lm & 1)) & 3;
      if (isY) tg = p1_loader<false>(lds, ws, x, (type ? wd_im : wd_re) + (size_t)pn * 128 * D_, w_in + zt * 128, r2, lm, pm, pn, c, lw, lane);
      else tg = p1_loader<true>(lds, ws, x, wd_re, w_in + FH + pn * 128, r2, lm, pm, pn, c, lw, lane);
      if (lw == 0) {
        wait_flags(ws + WS_FLG + (size_t)(c * 64 + lane) * 8, tg);
        *(volatile LAS unsigned*)(lds + T_EPO + 8) = 1u;
      } else {
#pragma unroll 1
        for (int spin = 0; spin < (SPIN << 4) && __builtin_amdgcn_readfirstlane(*(volatile LAS unsigned*)(lds + T_EPO + 8)) == 0u; ++spin) __builtin_amdgcn_s_sleep(1);
      }
      MEMFENCE();
      r2.P(0, r2.ord(0)); r2.P(1, r2.ord(1));
      WAIT_V(4); BAR();
      r2.P(2, r2.ord(2)); r2.P(3, r2.ord(3));
      WAIT_V(8); BAR(); MEMFENCE();
      { const int kk = r2.ord(4); r2.P(0, kk); r2.Q(0, kk); }
      WAIT_V(12); BAR(); MEMFENCE();
      int o[4];
#pragma unroll
      for (int v = 0; v < 4; ++v) o[v] = r2.ord(5 + v);
      const rsrc_t rZ = mk_rsrc(ws + WS_Z1 + (size_t)pm * 8 * 28 * 1024, 8 * 28 * 1024);
      const char* zf = ws + WS_FLG + (size_t)((15 + zt) * 64 + pm * 4 + (lane & 3)) * 8;
      u32x2 zfl;
#pragma unroll 1
      for (int u = 1; u < 7; ++u) {
        static_for<4>([&](auto V) {
          constexpr int v = V.value;
          r2.P((v + 1) & 3, o[v]); r2.Q((v + 1) & 3, o[v]);
          o[v] += 4;
          if constexpr (v == 0) {
            if (!isY && u >= 2) {
              if (u == 2) asm volatile("global_load_dwordx2 %0, %1, off sc1" : "=&v"(zfl) : "v"(zf) : "memory");
              else {
                if (u == 3) {
                  asm volatile("" : "+v"(zfl));
                  if (!__all(zfl.x == tg.a && zfl.y == tg.b)) wait_flags(zf, tg);
                }
#pragma unroll
                for (int i = 0; i < 2; ++i)
                  DMA16D(rZ, lds + A_TAB + (((u - 3) * 2 + i) * 4 + lw) * 1024, lane * 16, (((u - 3) * 2 + i) * 28 + pn * 4 + lw) * 1024);
              }
            }
          }
          WAIT_V(16); BAR(); MEMFENCE();
        });
      }
      static_for<3>([&](auto V) {
        const int kk = r2.ord(29 + V.value);
        r2.P((V.value + 1) & 3, kk); r2.Q((V.value + 1) & 3, kk);
        WAIT_V(16); BAR(); MEMFENCE();
      });
      WAIT_V(8); BAR(); MEMFENCE();
      issW(0, 4);
      WAIT_V(16); BAR();
      BAR();
    } else issW(0, 4);
    BAR();
    MEMFENCE();
    issW(4, 7);
    WAIT_V(0);
    BAR();
    if (lw == 0) {
      const int l = lane & 15;
      if (spare) __builtin_amdgcn_s_sleep(127);
      wait_flags(ws + WS_FL3 + (size_t)((pm * 2 + hb) * 32 + (l < 14 ? l : 16 + (l & 1) * 8 + on)) * 8, tg);
    }
    BAR();
    MEMFENCE();
    const rsrc_t rH = mk_rsrc(ws + WS_H + (size_t)(pm * 8 + hb * 4 + lw) * 28 * 1024, 28 * 1024);
    static_for<7>([&](auto B) {
      constexpr int b = B.value, s0 = 2 * b < 7 ? 2 * b : 7, s1 = 2 * b + 2 < 7 ? 2 * b + 2 : 7;
      static_for<s1 - s0>([&](auto T) {
        const int kk = ord3(s0 + T.value);
#pragma unroll
        for (int i = 0; i < 4; ++i) DMA16D(rH, lds + (s0 + T.value) * 16384 + lw * 4096 + i * 1024, lane * 16, kk * 4096 + i * 1024);
      });
      WAIT_V(4 * (s1 - b - 1));
      BAR();
    });
    asm volatile(".Lfm_e1:");
    return;
  }

  if (!spare) {
    f32x4 acc[4][4];
    unsigned bsr[4] = {0u, 0u, 0u, 0u};
    STAMP(0, 0);
    const u64 epoch = load_epoch(ws);
    if (isY) {
      {
        const int row0 = (xcd * 32 + idx) * 16 + w * 4;
        u32x4 v[4];
        load_row4(v, x + (size_t)row0 * D_, lane);
        const float mu = row4_sum(v) * (1.0f / D_);
        const f32x2 m2 = {mu, mu};
        f32x2 q2 = {0.f, 0.f};
#pragma unroll
        for (int i = 0; i < 4; ++i)
#pragma unroll
          for (int e = 0; e < 4; ++e) { const f32x2 d = up2(v[i][e]) - m2; q2 = __builtin_elementwise_fma(d, d, q2); }
        const float rstd = __builtin_amdgcn_rsqf(dpp_add16(q2.x + q2.y) * (1.0f / D_) + 1e-5f);
        tg = next_tags(epoch);
        if (fr == 0) st16<ST_AUX>((u32x4){__float_as_uint(mu), tg.a, __float_as_uint(rstd), tg.b}, mk_rsrc(ws + WS_ROW, S_ * 16), (row0 + fq) * 16);
        if (w == 0 && lane == 0) { *(LAS u64*)(lds + T_EPO) = epoch; *(LAS unsigned*)(lds + T_EPO + 8) = 0u; }
      }
      asm volatile("s_waitcnt lgkmcnt(0)" ::: "memory");
      BAR();
      SCHED();
      STAMP(0, 1);
      p1_loop<0>(lds, acc, w, lane, [&](int k) { STEPSTAMP(k); });
      STAMP(0, 2);
      BAR();
      MEMFENCE();
      {
        RowLN r;
        read_rowln(r, lds, wi, fq);
        const rsrc_t rO = mk_rsrc(ws + WS_CT + (size_t)c * 8 * 65536 + pm * 4096, 8 * 65536);
#pragma unroll
        for (int n = 0; n < 4; ++n) {
          const float c1 = *(LAS float*)(lds + T_COL + (wj * 64 + n * 16 + fr) * 4);
          const f32x2 cc = {c1, c1};
#pragma unroll
          for (int h = 0; h < 2; ++h) {
            const u32x4 o = {pk2(ln_val(r, acc, n, h, 0, cc)), pk2(ln_val(r, acc, n, h, 1, cc)), pk2(ln_val(r, acc, n, h, 2, cc)), pk2(ln_val(r, acc, n, h, 3, cc))};
            st16<ST_AUX>(o, rO, lane * 16 + (wj * 4 + n) * 65536 + (wi * 2 + h) * 1024);
          }
        }
      }
      asm volatile("s_waitcnt lgkmcnt(0)" ::: "memory");
      BAR();
      SCHED();
      STAMP(0, 3);
      p1_loop<1>(lds, acc, w, lane, [&](int k) { if (k == KPUB) publish_flag(ws, c, pm, w, lane, tg); });
      STAMP(0, 4);
      BAR();
      MEMFENCE();
      const rsrc_t rO = mk_rsrc(ws + WS_Z1 + (size_t)pm * 8 * 28 * 1024, c < 7 ? 8 * 28 * 1024 : 0);
#pragma unroll
      for (int h = 0; h < 2; ++h) {
        const f32x4 c0 = *(LAS f32x4*)(lds + T_COL + (wj * 64 + (h ^ wi) * 32 + 4 * fq) * 4);
        const f32x4 c1 = *(LAS f32x4*)(lds + T_COL + (wj * 64 + (h ^ wi) * 32 + 16 + 4 * fq) * 4);
#pragma unroll
        for (int m = 0; m < 4; ++m) {
          const f32x2 mr = *(LAS f32x2*)(lds + T_ROW + (wi * 64 + m * 16 + fr) * 8);
          const f32x2 nm = {-mr.x, -mr.x}, rs = {mr.y, mr.y};
          const f32x4 v0 = acc[m][2 * h], v1 = acc[m][2 * h + 1];
          u32x4 o;
          o.x = pk2(gelu2(rs * __builtin_elementwise_fma(nm, lo2(c0), lo2(v0))));
          o.y = pk2(gelu2(rs * __builtin_elementwise_fma(nm, hi2(c0), hi2(v0))));
          o.z = pk2(gelu2(rs * __builtin_elementwise_fma(nm, lo2(c1), lo2(v1))));
          o.w = pk2(gelu2(rs * __builtin_elementwise_fma(nm, hi2(c1), hi2(v1))));
          st16<ST_AUX>(o, rO, lane * 16 + ((wi * 4 + m) * 28 + zt * 4 + wj * 2 + (h ^ wi)) * 1024);
        }
      }
      STAMP(0, 5);
    } else {
#pragma unroll
      for (int n = 0; n < 4; ++n) bsr[n] = b_sp[pm * 128 + wj * 64 + n * 16 + fr];
      if (w == 0 && lane == 0) { *(LAS u64*)(lds + T_EPO) = epoch; *(LAS unsigned*)(lds + T_EPO + 8) = 0u; }
      asm volatile("s_waitcnt lgkmcnt(0)" : "+v"(bsr[0]), "+v"(bsr[1]), "+v"(bsr[2]), "+v"(bsr[3]) :: "memory");
      tg = next_tags(epoch);
      BAR();
      SCHED();
      STAMP(0, 1);
      p1_loop<2>(lds, acc, w, lane, [&](int k) { STEPSTAMP(k); });
      STAMP(0, 2);
      BAR();
      MEMFENCE();
      f32x2 z[4][2][4];
      {
        RowLN r;
        read_rowln(r, lds, wi, fq);
        f32x2 s1[2][4], s2[2][4];
#pragma unroll
        for (int n = 0; n < 4; ++n) {
          const float c1 = *(LAS float*)(lds + T_COL + (wj * 64 + (n ^ (2 * wi)) * 16 + fr) * 4);
          const f32x2 cc = {c1, c1};
#pragma unroll
          for (int h = 0; h < 2; ++h)
#pragma unroll
            for (int e = 0; e < 4; ++e) {
              const f32x2 g = gelu2(ln_val(r, acc, n, h, e, cc));
              z[n][h][e] = g;
              if (n == 0) { s1[h][e] = g; s2[h][e] = g * g; } else { s1[h][e] += g; s2[h][e] = __builtin_elementwise_fma(g, g, s2[h][e]); }
            }
        }
#pragma unroll
        for (int h = 0; h < 2; ++h) {
          const float v[16] = {s1[h][0].x, s2[h][0].x, s1[h][0].y, s2[h][0].y, s1[h][1].x, s2[h][1].x, s1[h][1].y, s2[h][1].y,
                               s1[h][2].x, s2[h][2].x, s1[h][2].y, s2[h][2].y, s1[h][3].x, s2[h][3].x, s1[h][3].y, s2[h][3].y};
          *(LAS float*)(lds + T_PART + ((wj * 128 + wi * 64 + 32 * h + 8 * fq) * 2 + fr) * 4) = reduce_scatter16(v, fr);
        }
      }
      asm volatile("s_waitcnt lgkmcnt(0)" ::: "memory");
      BAR();
      MEMFENCE();
      if (w < 2) {
        const int row = w * 64 + lane;
        const f32x2 p = *(LAS f32x2*)(lds + T_PART + row * 8) + *(LAS f32x2*)(lds + T_PART + (128 + row) * 8);
        st16<ST_AUX>((u32x4){__float_as_uint(p.x), tg.a, __float_as_uint(p.y), tg.b}, mk_rsrc(ws + WS_STAT + (size_t)pm * 7 * 2048, 7 * 2048), row * 16 + pn * 2048);
      }
      STAMP(0, 3);
      BAR();
      MEMFENCE();
      STAMP(0, 4);
      const rsrc_t rO = mk_rsrc(ws + WS_CT + (size_t)c * 8 * 65536 + pm * 4096, 8 * 65536);
#pragma unroll
      for (int h = 0; h < 2; ++h) {
        f32x2 nm[4], rs[4];
#pragma unroll
        for (int e = 0; e < 4; ++e) {
          const int ro = (wi * 64 + 32 * h + 8 * fq + 2 * e) * 8;
          const f32x4 u = *(LAS f32x4*)(lds + T_FIN + ro) + *(LAS f32x4*)(lds + T_FIN + 1024 + ro);
          const f32x2 mu = (f32x2){u.x, u.z} * (1.0f / FH);
          const f32x2 var = (f32x2){u.y, u.w} * (1.0f / FH) - mu * mu;
          nm[e] = -mu;
          rs[e] = (f32x2){__builtin_amdgcn_rsqf(fmaxf(var.x, 0.f) + 1e-5f), __builtin_amdgcn_rsqf(fmaxf(var.y, 0.f) + 1e-5f)};
        }
#pragma unroll
        for (int n = 0; n < 4; ++n) {
          u32x4 o;
#pragma unroll
          for (int e = 0; e < 4; ++e) o[e] = pk2((z[n][h][e] + nm[e]) * rs[e]);
          st16<ST_AUX>(o, rO, lane * 16 + (wj * 4 + (n ^ (2 * wi))) * 65536 + (wi * 2 + h) * 1024);
        }
      }
      STAMP(0, 5);
      publish_flag(ws, c, pm, w, lane, tg);
      STAMP(0, 6);
    }
    p2_loop(lds, acc, w, lane, ws, [&](int it) { if (it == 0 && c < 7) publish_flag(ws, 15 + c, pm, w, lane, tg); });
    STAMP(1, 2);
    if (isY) {
      const rsrc_t rO = mk_rsrc(ws + WS_Y + ((size_t)type * 128 + pm * 8) * 16 * 1024, 8 * 16 * 1024);
#pragma unroll
      for (int n = 0; n < 4; ++n)
#pragma unroll
        for (int h = 0; h < 2; ++h) {
          const f32x4 v0 = acc[2 * h][n], v1 = acc[2 * h + 1][n];
          const u32x4 o = {pk2(lo2(v0)), pk2(hi2(v0)), pk2(lo2(v1)), pk2(hi2(v1))};
          st16<ST_AUX>(o, rO, lane * 16 + ((wj * 4 + n) * 16 + pn * 4 + wi * 2 + h) * 1024);
        }
    } else {
      const rsrc_t rO = mk_rsrc(ws + WS_H + (size_t)pm * 8 * 28 * 1024, 8 * 28 * 1024);
#pragma unroll
      for (int n = 0; n < 4; ++n) {
        const float b1 = bflo(bsr[n]);
        const f32x2 b = {b1, b1};
#pragma unroll
        for (int h = 0; h < 2; ++h) {
          const f32x4 v0 = acc[2 * h][n], v1 = acc[2 * h + 1][n];
          const u32x4 zz = *(LAS u32x4*)(lds + A_TAB + ((wj * 4 + n) * 4 + wi * 2 + h) * 1024 + lane * 16);
          u32x4 o;
          o.x = pk2(up2(zz.x) * (lo2(v0) + b));
          o.y = pk2(up2(zz.y) * (hi2(v0) + b));
          o.z = pk2(up2(zz.z) * (lo2(v1) + b));
          o.w = pk2(up2(zz.w) * (hi2(v1) + b));
          st16<ST_AUX>(o, rO, lane * 16 + ((wj * 4 + n) * 28 + pn * 4 + wi * 2 + h) * 1024);
        }
      }
    }
    STAMP(1, 5);
  }
  asm volatile("s_waitcnt lgkmcnt(0)" ::: "memory");
  BAR();
  MEMFENCE();
  if (!spare) publish_at(ws + WS_FL3 + (size_t)((pm * 2 + wj) * 32 + (isY ? 16 + c * 2 + wi : pn * 2 + wi)) * 8, lane, tg);
  STAMP(1, 6);
  {
    const unsigned lb = (unsigned)(size_t)lds;
    const int tt0 = pm * 8 + hb * 4;
    u32x2 xv[4];
#pragma unroll
    for (int n = 0; n < 4; ++n) xv[n] = *(const u32x2*)(x + (size_t)((tt0 + n) * 16 + fr) * D_ + on * 64 + w * 16 + 4 * fq);
    u32x2 wf[28][2];
    BAR();
    SCHED();
    {
      const int row = 4 * fq + (fr >> 2);
      const unsigned a0 = lb + row * 128 + ((w ^ ((row >> 1) & 3)) << 5) + (fr & 3) * 8, a1 = a0 + 57344;
      static_for<28>([&](auto S) {
        constexpr int s = S.value;
        dstr64<(s % 14) * 4096>(wf[s][0], s < 14 ? a0 : a1);
        dstr64<(s % 14) * 4096 + 2048>(wf[s][1], s < 14 ? a0 : a1);
        if constexpr ((s & 3) == 3) WAIT_L0();
      });
    }
    BAR();
    MEMFENCE();
    SCHED();
    u32x2 yr[4], yi[4];
    f32x4 oc[4];
#pragma unroll
    for (int n = 0; n < 4; ++n) oc[n] = (f32x4){0.f, 0.f, 0.f, 0.f};
    const unsigned h0 = lb + lane * 16, h1 = h0 + 65536;
    bf16x8 qa[8], qb[8];
    auto rdq = [&](bf16x8 (&q)[8], auto I, auto HC) {
      constexpr int i = I.value, hc = HC.value, ch = hc >> 1;
      ds128<(ch & 3) * 16384 + (i & 3) * 4096 + (2 * (hc & 1) + (i >> 2)) * 1024>(q[i], ch < 4 ? h0 : h1);
    };
    auto halfc = [&](const bf16x8 (&q)[8], bf16x8 (&g)[8], auto HC) {
      constexpr int hc = HC.value;
      static_for<8>([&](auto I) {
        constexpr int i = I.value, s = 2 * hc + (i >> 2);
        if constexpr (hc + 1 < 14) rdq(g, I, IC<hc + 1>{});
        asm volatile("s_waitcnt lgkmcnt(%0)" ::"n"(hc + 1 < 14 ? 8 : 7 - i) : "memory");
        SCHED();
        oc[i & 3] = MFMA(join(wf[s][0], wf[s][1]), q[i], oc[i & 3]);
        SCHED();
      });
    };
    SCHED();
    BAR();
    SCHED();
    STAMP(2, 1);
    static_for<8>([&](auto I) { rdq(qa, I, IC<0>{}); });
    static_for<7>([&](auto CH) {
      constexpr int ch = CH.value;
      halfc(qa, qb, IC<2 * ch>{});
      if constexpr (ch < 6) { BAR(); SCHED(); }
      if constexpr (ch == 2) {
        const rsrc_t rY = mk_rsrc(ws + WS_Y, 2 * 128 * 16 * 1024);
#pragma unroll
        for (int n = 0; n < 4; ++n) {
          yr[n] = __builtin_amdgcn_raw_buffer_load_b64(rY, lane * 16 + wj * 8, ((tt0 + n) * 16 + on * 2 + wi) * 1024, 16);
          yi[n] = __builtin_amdgcn_raw_buffer_load_b64(rY, lane * 16 + wj * 8, ((128 + tt0 + n) * 16 + on * 2 + wi) * 1024, 16);
        }
        SCHED();
      }
      halfc(qb, qa, IC<2 * ch + 1>{});
    });
    STAMP(2, 2);
#pragma unroll
    for (int n = 0; n < 4; ++n) {
      u32x2 o;
      o.x = pk2(up2(xv[n].x) + (up2(yr[n].x) - up2(yi[n].x)) + lo2(oc[n]));
      o.y = pk2(up2(xv[n].y) + (up2(yr[n].y) - up2(yi[n].y)) + hi2(oc[n]));
      *(u32x2*)(out + (size_t)((tt0 + n) * 16 + fr) * D_ + on * 64 + w * 16 + 4 * fq) = o;
    }
    STAMP(2, 5);
    STAMP(2, 6);
    asm volatile(".Lfm_e2:");
  }
#endif
}

extern "C" void kernel_launch(void* const* d_in, const int* in_sizes, int n_in, void* d_out, int out_size, void* d_ws,
                              size_t ws_size, hipStream_t stream) {
  const u16* x = (const u16*)d_in[0];
  const u16* ws_re = (const u16*)d_in[1];
  const u16* ws_im = (const u16*)d_in[2];
  const u16* wd_re = (const u16*)d_in[3];
  const u16* wd_im = (const u16*)d_in[4];
  const u16* w_in = (const u16*)d_in[6];
  const u16* w_sp = (const u16*)d_in[7];
  const u16* b_sp = (const u16*)d_in[8];
  const u16* w_out = (const u16*)d_in[9];
  (void)hipFuncSetAttribute((const void*)fm_main, hipFuncAttributeMaxDynamicSharedMemorySize, A_LDS);
  fm_main<<<256, 512, A_LDS, stream>>>(x, wd_re, wd_im, w_in, ws_re, ws_im, w_sp, b_sp, w_out, (char*)d_ws, (u16*)d_out);
}
```

```cpp
#include <hip/hip_runtime.h>
#include <hip/hip_bf16.h>
#include <stdint.h>

typedef unsigned short u16;
typedef __bf16 bf16x8 __attribute__((ext_vector_type(8)));
typedef float f32x4 __attribute__((ext_vector_type(4)));
typedef float f32x2 __attribute__((ext_vector_type(2)));
typedef unsigned u32x4 __attribute__((ext_vector_type(4)));
typedef unsigned u32x2 __attribute__((ext_vector_type(2)));
typedef unsigned long long u64;
#define LAS __attribute__((address_space(3)))
#define GAS __attribute__((address_space(1)))
typedef __amdgpu_buffer_rsrc_t rsrc_t;

constexpr int S_ = 2048, D_ = 512, F_ = 1792, FH = 896;

constexpr size_t WS_EPOCH = 0;
constexpr size_t WS_STAT = 4096;
constexpr size_t WS_ROW = 512u << 10;
constexpr size_t WS_COL = 576u << 10;
constexpr size_t WS_FLG = 640u << 10;
constexpr size_t WS_FL3 = 672u << 10;
constexpr size_t WS_CT = 1u << 20;
constexpr size_t WS_Z1 = WS_CT + (size_t)120 * 65536;
constexpr size_t WS_H = WS_Z1 + (size_t)128 * 28 * 1024;
constexpr size_t WS_Y = WS_H + (size_t)128 * 28 * 1024;

#define ST_AUX 16

#define STAMP(kern, i) do {} while (0)
#define STEPSTAMP(k) do {} while (0)
template <int V> struct IC { static constexpr int value = V; };
template <int N, int I = 0, class F> __device__ __forceinline__ void static_for(F&& f) {
  if constexpr (I < N) { f(IC<I>{}); static_for<N, I + 1>(f); }
}

__device__ __forceinline__ float bflo(unsigned w) { return __uint_as_float(w << 16); }
__device__ __forceinline__ float bfhi(unsigned w) { return __uint_as_float(w & 0xffff0000u); }
__device__ __forceinline__ unsigned pk_bf16(float lo, float hi) {
  typedef __bf16 bf16x2 __attribute__((ext_vector_type(2)));
  f32x2 v = {lo, hi};
  return __builtin_bit_cast(unsigned, __builtin_convertvector(v, bf16x2));
}
__device__ __forceinline__ float gelu_tanh(float x) {
  const float c1 = -2.0f * 0.7978845608028654f * 1.4426950408889634f;
  const float c3 = c1 * 0.044715f;
  const float t = x * __builtin_fmaf(x * x, c3, c1);
  return x * __builtin_amdgcn_rcpf(1.0f + __builtin_amdgcn_exp2f(t));
}
__device__ __forceinline__ f32x2 gelu2(f32x2 x) {
  const float c1 = -2.0f * 0.7978845608028654f * 1.4426950408889634f;
  const float c3 = c1 * 0.044715f;
  const f32x2 t = x * __builtin_elementwise_fma(x * x, (f32x2){c3, c3}, (f32x2){c1, c1});
  f32x2 e = {__builtin_amdgcn_exp2f(t.x), __builtin_amdgcn_exp2f(t.y)};
  e = e + (f32x2){1.0f, 1.0f};
  return x * (f32x2){__builtin_amdgcn_rcpf(e.x), __builtin_amdgcn_rcpf(e.y)};
}
__device__ __forceinline__ f32x2 lo2(f32x4 v) { return (f32x2){v.x, v.y}; }
__device__ __forceinline__ f32x2 hi2(f32x4 v) { return (f32x2){v.z, v.w}; }
__device__ __forceinline__ unsigned pk2(f32x2 v) { return pk_bf16(v.x, v.y); }
__device__ __forceinline__ f32x2 up2(unsigned w) { return (f32x2){bflo(w), bfhi(w)}; }
__device__ __forceinline__ rsrc_t mk_rsrc(const void* p, unsigned bytes) {
  return __builtin_amdgcn_make_buffer_rsrc((void*)p, (short)0, (int)bytes, 0x00020000);
}
#define WAIT_V(n) asm volatile("s_waitcnt vmcnt(%0)" ::"n"(n) : "memory")
#define BAR() __builtin_amdgcn_s_barrier()
#define SCHED() __builtin_amdgcn_sched_barrier(0)
#define MEMFENCE() asm volatile("" ::: "memory")
#define WAIT_L0() do { asm volatile("s_waitcnt lgkmcnt(0)" ::: "memory"); SCHED(); } while (0)
#define DMA16(rs, ldsp, voff, soff) __builtin_amdgcn_raw_ptr_buffer_load_lds(rs, (LAS void*)(ldsp), 16, voff, soff, 0, 0)
#define DMA16D(rs, ldsp, voff, soff) __builtin_amdgcn_raw_ptr_buffer_load_lds(rs, (LAS void*)(ldsp), 16, voff, soff, 0, 16)
#define TOUCH(rs, ldsp, voff, soff) __builtin_amdgcn_raw_ptr_buffer_load_lds(rs, (LAS void*)(ldsp), 4, voff, soff, 0, 0)
#define MFMA(a, b, c) __builtin_amdgcn_mfma_f32_16x16x32_bf16(a, b, c, 0, 0, 0)

template <int AUX> __device__ __forceinline__ void st16(u32x4 v, rsrc_t r, int off) { __builtin_amdgcn_raw_buffer_store_b128(v, r, off, 0, AUX); }
template <int OFF> __device__ __forceinline__ void ds128(bf16x8& d, unsigned a) {
  asm volatile("ds_read_b128 %0, %1 offset:%2" : "=v"(d) : "v"(a), "n"(OFF));
}
template <int OFF> __device__ __forceinline__ void dstr64(u32x2& d, unsigned a) {
  asm volatile("ds_read_b64_tr_b16 %0, %1 offset:%2" : "=v"(d) : "v"(a), "n"(OFF));
}
__device__ __forceinline__ bf16x8 join(const u32x2& lo, const u32x2& hi) {
  return __builtin_bit_cast(bf16x8, (u32x4){lo.x, lo.y, hi.x, hi.y});
}
__device__ __forceinline__ float dpp_add16(float v) {
  v += __builtin_bit_cast(float, __builtin_amdgcn_update_dpp(0, __builtin_bit_cast(int, v), 0x128, 0xf, 0xf, false));
  v += __builtin_bit_cast(float, __builtin_amdgcn_update_dpp(0, __builtin_bit_cast(int, v), 0x124, 0xf, 0xf, false));
  v += __builtin_bit_cast(float, __builtin_amdgcn_update_dpp(0, __builtin_bit_cast(int, v), 0x122, 0xf, 0xf, false));
  v += __builtin_bit_cast(float, __builtin_amdgcn_update_dpp(0, __builtin_bit_cast(int, v), 0x121, 0xf, 0xf, false));
  return v;
}

template <int CTRL> __device__ __forceinline__ float dpp_mov(float v) {
  return __builtin_bit_cast(float, __builtin_amdgcn_update_dpp(0, __builtin_bit_cast(int, v), CTRL, 0xf, 0xf, false));
}
__device__ __forceinline__ float reduce_scatter16(const float (&v)[16], int fr) {
  float a[8], b[4], c[2];
  const bool b3 = fr & 8, b2 = fr & 4, b1 = fr & 2, b0 = fr & 1;
#pragma unroll
  for (int i = 0; i < 8; ++i) a[i] = (b3 ? v[i + 8] : v[i]) + dpp_mov<0x128>(b3 ? v[i] : v[i + 8]);
#pragma unroll
  for (int i = 0; i < 4; ++i) b[i] = (b2 ? a[i + 4] : a[i]) + dpp_mov<0x141>(b2 ? a[i] : a[i + 4]);
#pragma unroll
  for (int i = 0; i < 2; ++i) c[i] = (b1 ? b[i + 2] : b[i]) + dpp_mov<0x4E>(b1 ? b[i] : b[i + 2]);
  return (b0 ? c[1] : c[0]) + dpp_mov<0xB1>(b0 ? c[0] : c[1]);
}
__device__ __forceinline__ float wave_sum(float s) { s = dpp_add16(s); s += __shfl_xor(s, 16); s += __shfl_xor(s, 32); return s; }

constexpr int A_TAB = 131072;
constexpr int T_ROW = A_TAB, T_COL = A_TAB + 1024, T_PART = A_TAB + 2048, T_FIN = A_TAB + 4096, T_EPO = A_TAB + 6144, T_JUNK = A_TAB + 7168;
constexpr int A_LDS = A_TAB + 32768;
constexpr int SPIN = 1 << 16;
#define KMID 4
#define KPUB 2
#define SPQ 0
#define SDELAY 16
#define FDELAY 24
#define PSLEEP 1
#define QDELAY 12

struct Tags { unsigned a, b; };
__device__ __forceinline__ Tags next_tags(u64 e) { return Tags{(unsigned)e + 1u, (unsigned)(e >> 32) + 0x9E3779B9u}; }
__device__ __forceinline__ u64 tag64(Tags t) { return ((u64)t.b << 32) | t.a; }
__device__ __forceinline__ u64 load_epoch(char* ws) { return __hip_atomic_load((GAS u64*)(ws + WS_EPOCH), __ATOMIC_RELAXED, __HIP_MEMORY_SCOPE_AGENT); }
__device__ __forceinline__ void ld16_issue(u32x4& g, const char* p) { asm volatile("global_load_dwordx4 %0, %1, off sc1" : "=&v"(g) : "v"(p) : "memory"); }
__device__ __forceinline__ u32x4 ld16_now(const char* p) {
  u32x4 g;
  asm volatile("global_load_dwordx4 %0, %1, off sc1\n\ts_waitcnt vmcnt(0)" : "=&v"(g) : "v"(p) : "memory");
  return g;
}
__device__ __forceinline__ u32x2 ld8_now(const char* p) {
  u32x2 g;
  asm volatile("global_load_dwordx2 %0, %1, off sc1\n\ts_waitcnt vmcnt(0)" : "=&v"(g) : "v"(p) : "memory");
  return g;
}
__device__ __forceinline__ int korder32(int t, int j, int vl) {
  if (t < 1) return j;
  const int t1 = t - 1;
  if (t1 < 28) { const int u = t1 >> 2, v = t1 & 3; return v == vl ? 4 * (u + 1) + j : 4 * u + ((j + 1 + (v < vl ? v : v - 1)) & 3); }
  return 28 + ((j + 1 + (t1 - 28)) & 3);
}
__device__ __forceinline__ void load_row4(u32x4 (&v)[4], const u16* rows, int lane) {
#pragma unroll
  for (int i = 0; i < 4; ++i) v[i] = *(const u32x4*)(rows + (size_t)(lane >> 4) * D_ + i * 128 + (lane & 15) * 8);
}
__device__ __forceinline__ float row4_sum(const u32x4 (&v)[4]) {
  f32x2 s = {0.f, 0.f};
#pragma unroll
  for (int i = 0; i < 4; ++i) s += (up2(v[i].x) + up2(v[i].y)) + (up2(v[i].z) + up2(v[i].w));
  return dpp_add16(s.x + s.y);
}

template <int MODE, class Hook>
__device__ __forceinline__ void p1_loop(LAS char* lds, f32x4 (&acc)[4][4], int w, int lane, Hook&& hook) {
  const int wi = w >> 1, wj = w & 1, fr = lane & 15, fq = lane >> 4;
  const unsigned lb = (unsigned)(size_t)lds;
  constexpr int NC = MODE == 0 ? 0 : 2, NAW = MODE == 0 ? 2 : 4;
  unsigned aX[2], aW[NAW];
#pragma unroll
  for (int ks = 0; ks < 2; ++ks) {
    aX[ks] = lb + (((wi * 64 + fr) * 128 + ((fq ^ (fr >> 1)) << 4)) ^ (ks * 64));
    if constexpr (MODE == 0) aW[ks] = lb + 16384 + (((wj * 64 + fr) * 128 + ((fq ^ (fr >> 1)) << 4)) ^ (ks * 64));
  }
  if constexpr (MODE != 0) {
    const int tq = fr >> 2, tp = fr & 3, tf = ((tq >> 1) & 1) | ((fq & 1) << 1);
#pragma unroll
    for (int n = 0; n < 4; ++n) aW[n] = lb + 16384 + wj * 8192 + (8 * fq + tq) * 128 + ((n ^ (2 * wi) ^ tf) << 5) + tp * 8;
  }
  f32x4 cs[2];
#pragma unroll
  for (int m = 0; m < 4; ++m)
#pragma unroll
    for (int n = 0; n < 4; ++n) acc[m][n] = (f32x4){0.f, 0.f, 0.f, 0.f};
  cs[0] = (f32x4){0.f, 0.f, 0.f, 0.f}; cs[1] = cs[0];
  bf16x8 ones = __builtin_bit_cast(bf16x8, (u32x4){0x3f803f80u, 0x3f803f80u, 0x3f803f80u, 0x3f803f80u});
  asm volatile("" : "+v"(ones));
  struct Frag { bf16x8 x[4]; bf16x8 w[MODE == 0 ? 4 : 1]; u32x2 t[MODE == 0 ? 1 : 4][2]; };
  Frag fa, fb;
  constexpr int NR = MODE == 0 ? 8 : 12, NM = 16 + NC;
  auto rd = [&](Frag& f, auto I, auto KS) {
    constexpr int i = I.value, ks = KS.value;
    if constexpr (i < 4) ds128<i * 2048>(f.x[i], aX[ks]);
    else if constexpr (MODE == 0) ds128<(i - 4) * 2048>(f.w[i - 4], aW[ks]);
    else { constexpr int n = (i - 4) >> 1, t = (i - 4) & 1; dstr64<(32 * ks + 4 * t) * 128>(f.t[n][t], aW[n]); }
  };
  auto wfrag = [&](const Frag& f, int n) -> bf16x8 { if constexpr (MODE == 0) return f.w[n]; else return join(f.t[n][0], f.t[n][1]); };
  int dS = 32768;
  auto half = [&](const Frag& f, Frag& g, auto KS) {
    static_for<NM>([&](auto I) {
      constexpr int i = I.value;
      if constexpr (i < 16) {
        constexpr int n = i >> 2, m = i & 3;
        if constexpr (MODE == 1) acc[m][n] = MFMA(wfrag(f, n), f.x[m], acc[m][n]);
        else acc[m][n] = MFMA(f.x[m], wfrag(f, n), acc[m][n]);
      } else cs[i - 16] = MFMA(ones, wfrag(f, i - 16), cs[i - 16]);
      SCHED();
      if constexpr (i < NR) { rd(g, I, KS); SCHED(); }
      else if constexpr (KS.value == 1 && i - NR < 2 + NAW) {
        if constexpr (i - NR < 2) aX[i - NR] += dS; else aW[i - NR - 2] += dS;
        SCHED();
      }
    });
    WAIT_L0();
  };
  static_for<NR>([&](auto I) { rd(fa, I, IC<0>{}); });
  WAIT_L0();
#pragma unroll 1
  for (int k = 0; k < 8; ++k) {
    dS = (k & 3) == 3 ? -98304 : 32768;
    half(fa, fb, IC<1>{});
    BAR();
    SCHED();
    half(fb, fa, IC<0>{});
    hook(k);
    SCHED();
  }
  if constexpr (NC > 0) {
    LAS float* colt = (LAS float*)(lds + T_COL);
    if (fq == 0) { colt[wj * 64 + wi * 32 + fr] = cs[0][0]; colt[wj * 64 + wi * 32 + 16 + fr] = cs[1][0]; }
  }
  asm volatile("s_waitcnt lgkmcnt(0)" ::: "memory");
}

template <class Hook>
__device__ __forceinline__ void p2_loop(LAS char* lds, f32x4 (&acc)[4][4], int w, int lane, char* ws, Hook&& hook) {
  const int wi = w >> 1, wj = w & 1, fr = lane & 15, fq = lane >> 4;
  const unsigned lb = (unsigned)(size_t)lds;
  unsigned aP[2], aQ[2][2];
#pragma unroll
  for (int sp = 0; sp < 2; ++sp) {
    aP[sp] = lb + sp * 65536 + wi * 8192 + lane * 16;
#pragma unroll
    for (int ks = 0; ks < 2; ++ks) aQ[sp][ks] = lb + sp * 65536 + 16384 + (((wj * 64 + fr) * 128 + ((fq ^ (fr >> 1)) << 4)) ^ (ks * 64));
  }
#pragma unroll
  for (int m = 0; m < 4; ++m)
#pragma unroll
    for (int n = 0; n < 4; ++n) acc[m][n] = (f32x4){0.f, 0.f, 0.f, 0.f};
  bf16x8 fa[8], fb[8];
  auto rd = [&](bf16x8 (&f)[8], auto I, auto S, auto KS) {
    constexpr int x = I.value, s = S.value, ks = KS.value;
    if constexpr (x < 4) ds128<(s & 1) * 32768 + (x * 2 + ks) * 1024>(f[x], aP[s >> 1]);
    else ds128<(s & 1) * 32768 + (x - 4) * 2048>(f[x], aQ[s >> 1][ks]);
  };
  auto half = [&](const bf16x8 (&f)[8], bf16x8 (&g)[8], auto S, auto KS) {
    static_for<16>([&](auto I) {
      constexpr int i = I.value;
      acc[i >> 2][i & 3] = MFMA(f[i >> 2], f[4 + (i & 3)], acc[i >> 2][i & 3]);
      SCHED();
      if constexpr (i < 8) { rd(g, I, S, KS); SCHED(); }
    });
    WAIT_L0();
  };
  BAR();
  SCHED();
  STAMP(1, 1);
  static_for<8>([&](auto I) { rd(fa, I, IC<0>{}, IC<0>{}); });
  WAIT_L0();
#pragma unroll 1
  for (int it = 0; it < 8; ++it) {
    static_for<4>([&](auto S) {
      half(fa, fb, S, IC<1>{});
      if (S.value < 3 || it < 7) BAR();
      SCHED();
      half(fb, fa, IC<(S.value + 1) & 3>{}, IC<0>{});
    });
    hook(it);
    SCHED();
  }
}

struct RowLN { f32x2 nm[2][4], rs[2][4]; };
__device__ __forceinline__ void read_rowln(RowLN& r, LAS char* lds, int wi, int fq) {
#pragma unroll
  for (int h = 0; h < 2; ++h)
#pragma unroll
    for (int e = 0; e < 4; ++e) {
      const f32x4 t = *(LAS f32x4*)(lds + T_ROW + (wi * 64 + 32 * h + 8 * fq + 2 * e) * 8);
      r.nm[h][e] = (f32x2){-t.x, -t.z}; r.rs[h][e] = (f32x2){t.y, t.w};
    }
}
__device__ __forceinline__ f32x2 ln_val(const RowLN& r, const f32x4 (&acc)[4][4], int n, int h, int e, f32x2 c) {
  const f32x4 v = acc[2 * h + (e >> 1)][n];
  return r.rs[h][e] * __builtin_elementwise_fma(r.nm[h][e], c, (e & 1) ? hi2(v) : lo2(v));
}
__device__ __forceinline__ void publish_at(char* p, int lane, Tags tg) {
  WAIT_V(0);
  if (lane == 0) __hip_atomic_store((GAS u64*)p, tag64(tg), __ATOMIC_RELAXED, __HIP_MEMORY_SCOPE_AGENT);
}
__device__ __forceinline__ void publish_flag(char* ws, int c, int pm, int w, int lane, Tags tg) {
  publish_at(ws + WS_FLG + (size_t)(c * 64 + pm * 4 + w) * 8, lane, tg);
}
__device__ __forceinline__ void wait_flags(const char* fp, Tags tg) {
#pragma unroll 1
  for (int spin = 0; spin < SPIN; ++spin) {
    const u32x2 fl = ld8_now(fp);
    if (__all(fl.x == tg.a && fl.y == tg.b)) break;
    __builtin_amdgcn_s_sleep(PSLEEP);
  }
}

struct Ring2 {
  rsrc_t rQ, rP; unsigned voffQ, voffP; LAS char* lw_lds; int j, vl;
  __device__ __forceinline__ void Q(int buf, int kk) const {
#pragma unroll
    for (int i = 0; i < 4; ++i) DMA16(rQ, lw_lds + buf * 32768 + 16384 + i * 4096, voffQ, kk * 128 + i * 32 * S_ * 2);
  }
  __device__ __forceinline__ void P(int buf, int kk) const {
#pragma unroll
    for (int i = 0; i < 4; ++i) DMA16D(rP, lw_lds + buf * 32768 + i * 4096, voffP, kk * 2048 + i * 131072);
  }
  __device__ __forceinline__ int ord(int t) const { return korder32(t, j, vl); }
};

template <bool G>
__device__ __forceinline__ Tags p1_loader(LAS char* lds, char* ws, const u16* x, const u16* wd, const u16* win, const Ring2& r2,
                                          int k0, int pm, int pn, int c, int lw, int lane) {
  constexpr int NK = G ? 8 : 16;
  const int rho = 8 * lw + (lane >> 3);
  const unsigned sw = (unsigned)(((lane & 7) ^ ((rho >> 1) & 7)) << 4);
  const rsrc_t rX = mk_rsrc(x + (size_t)pm * 128 * D_, 128 * D_ * 2);
  const unsigned voffXp = (unsigned)((8 * ((rho >> 2) & 3) + 4 * (rho >> 4) + (rho & 3)) * (D_ * 2)) + sw;
  const unsigned voffXn = (unsigned)(rho * (D_ * 2)) + sw;
  const rsrc_t rW = mk_rsrc(win, (D_ - 1) * F_ * 2 + 256), rD = mk_rsrc(wd, 128 * D_ * 2);
  const int f = ((rho >> 1) & 1) | (((rho >> 3) & 1) << 1);
  const unsigned voffW = (unsigned)(rho * (F_ * 2) + (((((lane & 7) >> 1) ^ f) << 5) | ((lane & 1) << 4)));
  LAS char* lw_lds = lds + lw * 1024;
  auto p1part = [&](auto H) {
    constexpr int h = H.value, t = h >> 1;
    const int kk = (t + k0) & 7;
    LAS char* p = lw_lds + (t & 3) * 32768;
    if constexpr ((h & 1) == 0) {
#pragma unroll
      for (int i = 0; i < 4; ++i) DMA16(rX, p + i * 4096, t < 8 ? voffXp : voffXn, kk * 128 + i * 32 * D_ * 2);
    } else if constexpr (!G && t < 8) {
#pragma unroll
      for (int i = 0; i < 4; ++i) DMA16(rD, p + 16384 + i * 4096, voffXn, kk * 128 + i * 32 * D_ * 2);
    } else {
#pragma unroll
      for (int i = 0; i < 4; ++i) DMA16(rW, p + 16384 + i * 4096, voffW, (kk * 64 + (i & 1) * 32) * (F_ * 2) + (i >> 1) * 128);
    }
  };
  constexpr int BG = G ? 6 : 5;
  constexpr auto upto = [](int b) constexpr { int h = 3 + 3 * b, lim = b == 0 ? 8 : 2 * (b + 3); h = h < lim ? h : lim; return h < 2 * NK ? h : 2 * NK; };
  constexpr auto after = [upto](int hx, int b) constexpr {
    int n = 0;
    for (int s = 0; s <= b; ++s) {
      for (int h = s ? upto(s - 1) : 0; h < upto(s); ++h) n = h == hx ? 0 : n + 4;
      n += s == BG ? 1 : 0;
      if (!G && s >= 14) n += 4;
    }
    return n;
  };
  const int row = (lw & 1) * 64 + lane;
  const char* gp = ws + (G || lw < 2 ? WS_ROW + (size_t)(pm * 128 + row) * 16 : WS_COL + (size_t)(c * 64 + lane) * 16);
  u32x4 g;
  Tags tg = {0u, 0u};
  auto table1 = [&]() {
    asm volatile("" : "+v"(g));
    tg = next_tags(*(LAS u64*)(lds + T_EPO));
    if (lw < (G ? 2 : 3)) {
#pragma unroll 1
      for (int spin = 0; spin < SPIN; ++spin) {
        if (__all(g.y == tg.a && g.w == tg.b)) break;
        __builtin_amdgcn_s_sleep(2);
        g = ld16_now(gp);
      }
      *(LAS f32x2*)(lds + (lw < 2 ? T_ROW + row * 8 : T_COL + lane * 8)) = (f32x2){__uint_as_float(g.x), __uint_as_float(g.z)};
      asm volatile("s_waitcnt lgkmcnt(0)" ::: "memory");
    }
  };
  static_for<NK>([&](auto B) {
    constexpr int b = B.value, h0 = b ? upto(b - 1) : 3, h1 = upto(b);
    static_for<h1 - h0>([&](auto H) { p1part(IC<h0 + H.value>{}); });
    if constexpr (b == BG) ld16_issue(g, gp);
    if constexpr (!G && b == 9) {
      WAIT_V(32);
      table1();
      BAR();
      WAIT_V(after(17, 9));
      BAR();
    }
    if constexpr (!G && b >= 14) r2.Q(b - 14, r2.ord(b - 14));
    if constexpr (!(!G && b == 8)) WAIT_V(after(2 * b + 1, b));
    BAR();
    MEMFENCE();
  });
  if constexpr (G) {
    BAR();
    WAIT_V(0);
    table1();
    BAR();
    r2.Q(0, r2.ord(0)); r2.Q(1, r2.ord(1)); r2.Q(2, r2.ord(2)); r2.Q(3, r2.ord(3));
    BAR();
    const int r2w = lw * 32 + (lane & 31), h2 = lane >> 5;
    const char* sp = ws + WS_STAT + (size_t)pm * 7 * 2048 + h2 * 3 * 2048 + r2w * 16;
    u32x4 q[4];
    __builtin_amdgcn_s_sleep(SDELAY);
#pragma unroll 1
    for (int spin = 0; spin < SPIN; ++spin) {
#pragma unroll
      for (int i = 0; i < 4; ++i) ld16_issue(q[i], sp + i * 2048);
      asm volatile("s_waitcnt vmcnt(0)" : "+v"(q[0]), "+v"(q[1]), "+v"(q[2]), "+v"(q[3]) :: "memory");
      bool ok = true;
#pragma unroll
      for (int i = 0; i < 4; ++i) ok = ok && q[i].y == tg.a && q[i].w == tg.b;
      if (__all(ok)) break;
    }
    float t1 = __uint_as_float(q[1].x) + __uint_as_float(q[2].x) + __uint_as_float(q[3].x) + (h2 ? 0.f : __uint_as_float(q[0].x));
    float t2 = __uint_as_float(q[1].z) + __uint_as_float(q[2].z) + __uint_as_float(q[3].z) + (h2 ? 0.f : __uint_as_float(q[0].z));
    t1 += __shfl_xor(t1, 32); t2 += __shfl_xor(t2, 32);
    const float mu = t1 * (1.0f / FH);
    const float rstd = __builtin_amdgcn_rsqf(fmaxf(t2 * (1.0f / FH) - mu * mu, 0.f) + 1e-5f);
    *(LAS float*)(lds + T_FIN + h2 * 512 + r2w * 4) = h2 ? rstd : -mu;
    asm volatile("s_waitcnt lgkmcnt(0)" ::: "memory");
    BAR();
    __builtin_amdgcn_s_sleep(FDELAY);
  } else {
    r2.Q(2, r2.ord(2));
    BAR();
    r2.Q(3, r2.ord(3));
    BAR();
  }
  return tg;
}

__global__ __launch_bounds__(512, 2) void fm_main(const u16* __restrict__ x, const u16* __restrict__ wd_re,
                                                  const u16* __restrict__ wd_im, const u16* __restrict__ w_in,
                                                  const u16* __restrict__ ws_re, const u16* __restrict__ ws_im,
                                                  const u16* __restrict__ w_sp, const u16* __restrict__ b_sp,
                                                  const u16* __restrict__ w_out, char* ws, u16* __restrict__ out) {
#if defined(__HIP_DEVICE_COMPILE__)
  extern __shared__ __attribute__((aligned(16))) char smem[];
  LAS char* lds = (LAS char*)smem;
  const int w = __builtin_amdgcn_readfirstlane(threadIdx.x >> 6), lane = threadIdx.x & 63;
  const int xcd = blockIdx.x & 7, idx = blockIdx.x >> 3;
  const int wi = (w >> 1) & 1, wj = w & 1, fr = lane & 15, fq = lane >> 4;
  {
    u64 pc; int len1, len2;
    asm volatile("s_getpc_b64 %0\n.Lfm_pc:\n\ts_mov_b32 %1, .Lfm_e1-.Lfm_pc\n\ts_mov_b32 %2, .Lfm_e2-.Lfm_pc" : "=s"(pc), "=s"(len1), "=s"(len2));
    const unsigned len = (unsigned)(len1 > len2 ? len1 : len2);
    if (w == 3 && len < 65536u && (unsigned)idx * 8192u < len) TOUCH(mk_rsrc((const void*)pc, len), lds + T_JUNK, lane * 128, idx * 8192);
  }
  const bool isY = xcd < 4;
  const int type = xcd >> 1;
  const int lm = isY ? idx >> 2 : idx >> 3;
  const int pm = (isY ? (xcd & 1) * 8 : (xcd - 4) * 4) + lm;
  const int pn = idx & (isY ? 3 : 7);
  const bool spare = pn == 7;
  const int c = (isY ? type * 4 : 8) + pn;
  const int zt = !isY ? pn : c < 7 ? c : 6;
  const int hb = isY ? 1 : 0, on = isY ? c : pn;
  const int k0 = (isY ? lm + 2 * pn : 2 * lm + pn) & 7;
  if (__builtin_expect(w >= 4 && !spare, 1)) {
    const int rho = 8 * (w - 4) + (lane >> 3);
    const unsigned sw = (unsigned)(((lane & 7) ^ ((rho >> 1) & 7)) << 4);
    const rsrc_t rX = mk_rsrc(x + (size_t)pm * 128 * D_, 128 * D_ * 2);
    const unsigned voffX = (unsigned)((8 * ((rho >> 2) & 3) + 4 * (rho >> 4) + (rho & 3)) * (D_ * 2)) + sw;
    const rsrc_t rW = mk_rsrc(isY ? (type ? wd_im : wd_re) + (size_t)pn * 128 * D_ : w_in + FH + pn * 128, isY ? 128 * D_ * 2 : (D_ - 1) * F_ * 2 + 256);
    const int f = ((rho >> 1) & 1) | (((rho >> 3) & 1) << 1);
    const unsigned voffW = isY ? (unsigned)(rho * (D_ * 2)) + sw : (unsigned)(rho * (F_ * 2) + (((((lane & 7) >> 1) ^ f) << 5) | ((lane & 1) << 4)));
    const int wb = k0 * (isY ? 128 : 64 * F_ * 2), w1 = isY ? 32 * D_ * 2 : 32 * F_ * 2, w2 = isY ? 64 * D_ * 2 : 128;
    LAS char* p = lds + (w - 4) * 1024;
#pragma unroll
    for (int i = 0; i < 4; ++i) DMA16(rX, p + i * 4096, voffX, k0 * 128 + i * 32 * D_ * 2);
#pragma unroll
    for (int i = 0; i < 4; ++i) DMA16(rW, p + 16384 + i * 4096, voffW, wb + (i & 1) * w1 + (i >> 1) * w2);
#pragma unroll
    for (int i = 0; i < 4; ++i) DMA16(rX, p + 32768 + i * 4096, voffX, ((k0 + 1) & 7) * 128 + i * 32 * D_ * 2);
  }
  Tags tg = {0u, 0u};
  if (spare) {
    const int sid = pm;
    tg = next_tags(load_epoch(ws));
    const int r0 = sid * 64 + w * 8;
    const u16* src = (r0 < 512 ? wd_re : wd_im) + (size_t)(r0 & 511) * D_;
    u32x4 va[4], vb[4];
#pragma unroll
    for (int i = 0; i < 4; ++i) {
      va[i] = *(const u32x4*)(src + (size_t)(2 * fq) * D_ + i * 128 + fr * 8);
      vb[i] = *(const u32x4*)(src + (size_t)(2 * fq + 1) * D_ + i * 128 + fr * 8);
    }
    const float sa = row4_sum(va), sb = row4_sum(vb);
    if (fr == 0) st16<ST_AUX>((u32x4){__float_as_uint(sa), tg.a, __float_as_uint(sb), tg.b}, mk_rsrc(ws + WS_COL, 512 * 16), (r0 / 2 + fq) * 16);
    MEMFENCE();
    BAR();
    MEMFENCE();
    {
      const rsrc_t rA = mk_rsrc(w_in, D_ * F_ * 2), rB = mk_rsrc(w_sp + (size_t)(xcd - 4) * 512 * S_, 512 * S_ * 2), rC = mk_rsrc(w_out, FH * D_ * 2);
      if (w < 7) {
        const int id = (sid * 7 + w) * 64 + lane;
        TOUCH(rA, lds + T_JUNK, (id / 14) * (F_ * 2) + (id % 14) * 128, 0);
      }
#pragma unroll 1
      for (int ch = w; ch < SPQ; ch += 8) {
        const int id = (ch * 4 + lm) * 64 + lane;
        TOUCH(rB, lds + T_JUNK, ((id >> 1) & 511) * (S_ * 2) + (2 * (id >> 10) + (id & 1)) * 128, 0);
        if (ch == w + 16 && w < 7) TOUCH(rC, lds + T_JUNK, lane * 128, (sid * 7 + w) * 8192);
      }
    }
    if (sid == 0 && w == 0) {
#pragma unroll 1
      for (int spin = 0; spin < SPIN; ++spin) {
        u32x4 g[16];
#pragma unroll
        for (int i = 0; i < 16; ++i)
          ld16_issue(g[i], i < 8 ? ws + WS_COL + (size_t)(i * 64 + lane) * 16 : ws + WS_FLG + (size_t)(i < 15 ? (i - 8) * 64 + lane : 7 * 64 + (lane & 31)) * 16);
        WAIT_V(0);
        bool ok = true;
#pragma unroll
        for (int i = 0; i < 16; ++i) {
          asm volatile("" : "+v"(g[i]));
          ok = ok && (i < 8 ? (g[i].y == tg.a && g[i].w == tg.b) : (g[i].x == tg.a && g[i].y == tg.b && g[i].z == tg.a && g[i].w == tg.b));
        }
        if (__all(ok)) break;
        __builtin_amdgcn_s_sleep(32);
      }
      if (lane == 0) __hip_atomic_store((GAS u64*)(ws + WS_EPOCH), tag64(tg), __ATOMIC_RELAXED, __HIP_MEMORY_SCOPE_AGENT);
    }
    WAIT_V(0);
  }

  if (w >= 4) {
    const int lw = w - 4, rho = 8 * lw + (lane >> 3);
    LAS char* lw_lds = lds + lw * 1024;
    const rsrc_t rW = mk_rsrc(w_out + on * 64, (FH - 1) * D_ * 2 + 128);
    const unsigned voffW = (unsigned)(rho * (D_ * 2) + (((((lane & 7) >> 1) ^ ((rho >> 1) & 3)) << 5) | ((lane & 1) << 4)));
    const int k3 = on < 7 ? on : 0;
    auto ord3 = [&](int t) { return t + k3 >= 7 ? t + k3 - 7 : t + k3; };
    auto issW = [&](int t0, int t1) {
#pragma unroll 1
      for (int t = t0; t < t1; ++t) {
        const int kk = ord3(t);
#pragma unroll
        for (int i = 0; i < 4; ++i) DMA16(rW, lw_lds + t * 16384 + i * 4096, voffW, (kk * 128 + i * 32) * (D_ * 2));
      }
    };
    if (!spare) {
      Ring2 r2;
      r2.rQ = mk_rsrc((isY ? (type ? ws_im : ws_re) : w_sp) + (size_t)pm * 128 * S_, 128 * S_ * 2);
      r2.rP = mk_rsrc(ws + WS_CT + (size_t)c * 8 * 65536, 8 * 65536);
      r2.voffQ = (unsigned)(rho * (S_ * 2) + (((lane & 7) ^ ((rho >> 1) & 7)) << 4));
      r2.voffP = (unsigned)((lw >> 1) * 65536 + (lw & 1) * 1024 + lane * 16);
      r2.lw_lds = lw_lds;
      r2.j = (lm + pn) & 3;
      r2.vl = (isY ? (lm >> 2) * 2 + (pn & 1) : (pn >> 2) * 2 + (lm & 1)) & 3;
      if (isY) tg = p1_loader<false>(lds, ws, x, (type ? wd_im : wd_re) + (size_t)pn * 128 * D_, w_in + zt * 128, r2, k0, pm, pn, c, lw, lane);
      else tg = p1_loader<true>(lds, ws, x, wd_re, w_in + FH + pn * 128, r2, k0, pm, pn, c, lw, lane);
      if (lw == 0) {
        wait_flags(ws + WS_FLG + (size_t)(c * 64 + lane) * 8, tg);
        *(volatile LAS unsigned*)(lds + T_EPO + 8) = 1u;
      } else {
#pragma unroll 1
        for (int spin = 0; spin < (SPIN << 4) && __builtin_amdgcn_readfirstlane(*(volatile LAS unsigned*)(lds + T_EPO + 8)) == 0u; ++spin) __builtin_amdgcn_s_sleep(1);
      }
      MEMFENCE();
      r2.P(0, r2.ord(0)); r2.P(1, r2.ord(1));
      WAIT_V(4); BAR();
      r2.P(2, r2.ord(2)); r2.P(3, r2.ord(3));
      WAIT_V(8); BAR(); MEMFENCE();
      { const int kk = r2.ord(4); r2.P(0, kk); r2.Q(0, kk); }
      WAIT_V(12); BAR(); MEMFENCE();
      int o[4];
#pragma unroll
      for (int v = 0; v < 4; ++v) o[v] = r2.ord(5 + v);
      const rsrc_t rZ = mk_rsrc(ws + WS_Z1 + (size_t)pm * 8 * 28 * 1024, 8 * 28 * 1024);
      const char* zf = ws + WS_FLG + (size_t)((15 + zt) * 64 + pm * 4 + (lane & 3)) * 8;
      u32x2 zfl;
#pragma unroll 1
      for (int u = 1; u < 7; ++u) {
        static_for<4>([&](auto V) {
          constexpr int v = V.value;
          r2.P((v + 1) & 3, o[v]); r2.Q((v + 1) & 3, o[v]);
          o[v] += 4;
          if constexpr (v == 0) {
            if (!isY && u >= 2) {
              if (u == 2) asm volatile("global_load_dwordx2 %0, %1, off sc1" : "=&v"(zfl) : "v"(zf) : "memory");
              else {
                if (u == 3) {
                  asm volatile("" : "+v"(zfl));
                  if (!__all(zfl.x == tg.a && zfl.y == tg.b)) wait_flags(zf, tg);
                }
#pragma unroll
                for (int i = 0; i < 2; ++i)
                  DMA16D(rZ, lds + A_TAB + (((u - 3) * 2 + i) * 4 + lw) * 1024, lane * 16, (((u - 3) * 2 + i) * 28 + pn * 4 + lw) * 1024);
              }
            }
          }
          WAIT_V(16); BAR(); MEMFENCE();
        });
      }
      static_for<3>([&](auto V) {
        const int kk = r2.ord(29 + V.value);
        r2.P((V.value + 1) & 3, kk); r2.Q((V.value + 1) & 3, kk);
        WAIT_V(16); BAR(); MEMFENCE();
      });
      WAIT_V(8); BAR(); MEMFENCE();
      issW(0, 2);
      WAIT_V(8); BAR(); MEMFENCE();
      issW(2, 5);
    } else issW(0, 5);
    BAR();
    MEMFENCE();
    issW(5, 7);
    WAIT_V(0);
    BAR();
    const char* f3 = ws + WS_FL3 + (size_t)((pm * 2 + hb) * 32 + ((lane & 15) < 14 ? lane & 15 : 16 + (lane & 1) * 8 + on)) * 8;
    bool yseen = false;
    if (lw == 0) {
      if (spare) __builtin_amdgcn_s_sleep(127);
#pragma unroll 1
      for (int spin = 0; spin < SPIN; ++spin) {
        const u32x2 fl = ld8_now(f3);
        const u64 m = __ballot(fl.x == tg.a && fl.y == tg.b);
        yseen = (m & 0xC000u) == 0xC000u;
        if ((m & 0x3FFFu) == 0x3FFFu) break;
        __builtin_amdgcn_s_sleep(PSLEEP);
      }
    }
    BAR();
    MEMFENCE();
    const rsrc_t rH = mk_rsrc(ws + WS_H + (size_t)(pm * 8 + hb * 4 + lw) * 28 * 1024, 28 * 1024);
    static_for<7>([&](auto B) {
      constexpr int b = B.value, s0 = 2 * b < 7 ? 2 * b : 7, s1 = 2 * b + 2 < 7 ? 2 * b + 2 : 7;
      static_for<s1 - s0>([&](auto T) {
        const int kk = ord3(s0 + T.value);
#pragma unroll
        for (int i = 0; i < 4; ++i) DMA16D(rH, lds + (s0 + T.value) * 16384 + lw * 4096 + i * 1024, lane * 16, kk * 4096 + i * 1024);
      });
      if constexpr (b == 4) { if (lw == 0 && !yseen) wait_flags(f3, tg); }
      WAIT_V(4 * (s1 - b - 1));
      BAR();
    });
    asm volatile(".Lfm_e1:");
    return;
  }

  if (!spare) {
    f32x4 acc[4][4];
    unsigned bsr[4] = {0u, 0u, 0u, 0u};
    STAMP(0, 0);
    const u64 epoch = load_epoch(ws);
    if (isY) {
      {
        const int row0 = (xcd * 32 + idx) * 16 + w * 4;
        u32x4 v[4];
        load_row4(v, x + (size_t)row0 * D_, lane);
        const float mu = row4_sum(v) * (1.0f / D_);
        const f32x2 m2 = {mu, mu};
        f32x2 q2 = {0.f, 0.f};
#pragma unroll
        for (int i = 0; i < 4; ++i)
#pragma unroll
          for (int e = 0; e < 4; ++e) { const f32x2 d = up2(v[i][e]) - m2; q2 = __builtin_elementwise_fma(d, d, q2); }
        const float rstd = __builtin_amdgcn_rsqf(dpp_add16(q2.x + q2.y) * (1.0f / D_) + 1e-5f);
        tg = next_tags(epoch);
        if (fr == 0) st16<ST_AUX>((u32x4){__float_as_uint(mu), tg.a, __float_as_uint(rstd), tg.b}, mk_rsrc(ws + WS_ROW, S_ * 16), (row0 + fq) * 16);
        if (w == 0 && lane == 0) { *(LAS u64*)(lds + T_EPO) = epoch; *(LAS unsigned*)(lds + T_EPO + 8) = 0u; }
      }
      asm volatile("s_waitcnt lgkmcnt(0)" ::: "memory");
      BAR();
      SCHED();
      STAMP(0, 1);
      p1_loop<0>(lds, acc, w, lane, [&](int k) { STEPSTAMP(k); });
      STAMP(0, 2);
      BAR();
      MEMFENCE();
      {
        RowLN r;
        read_rowln(r, lds, wi, fq);
        const rsrc_t rO = mk_rsrc(ws + WS_CT + (size_t)c * 8 * 65536 + pm * 4096, 8 * 65536);
#pragma unroll
        for (int n = 0; n < 4; ++n) {
          const float c1 = *(LAS float*)(lds + T_COL + (wj * 64 + n * 16 + fr) * 4);
          const f32x2 cc = {c1, c1};
#pragma unroll
          for (int h = 0; h < 2; ++h) {
            const u32x4 o = {pk2(ln_val(r, acc, n, h, 0, cc)), pk2(ln_val(r, acc, n, h, 1, cc)), pk2(ln_val(r, acc, n, h, 2, cc)), pk2(ln_val(r, acc, n, h, 3, cc))};
            st16<ST_AUX>(o, rO, lane * 16 + (wj * 4 + n) * 65536 + (wi * 2 + h) * 1024);
          }
        }
      }
      asm volatile("s_waitcnt lgkmcnt(0)" ::: "memory");
      BAR();
      SCHED();
      STAMP(0, 3);
      p1_loop<1>(lds, acc, w, lane, [&](int k) { if (k == KPUB) publish_flag(ws, c, pm, w, lane, tg); });
      STAMP(0, 4);
      BAR();
      MEMFENCE();
      const rsrc_t rO = mk_rsrc(ws + WS_Z1 + (size_t)pm * 8 * 28 * 1024, c < 7 ? 8 * 28 * 1024 : 0);
#pragma unroll
      for (int h = 0; h < 2; ++h) {
        const f32x4 c0 = *(LAS f32x4*)(lds + T_COL + (wj * 64 + (h ^ wi) * 32 + 4 * fq) * 4);
        const f32x4 c1 = *(LAS f32x4*)(lds + T_COL + (wj * 64 + (h ^ wi) * 32 + 16 + 4 * fq) * 4);
#pragma unroll
        for (int m = 0; m < 4; ++m) {
          const f32x2 mr = *(LAS f32x2*)(lds + T_ROW + (wi * 64 + m * 16 + fr) * 8);
          const f32x2 nm = {-mr.x, -mr.x}, rs = {mr.y, mr.y};
          const f32x4 v0 = acc[m][2 * h], v1 = acc[m][2 * h + 1];
          u32x4 o;
          o.x = pk2(gelu2(rs * __builtin_elementwise_fma(nm, lo2(c0), lo2(v0))));
          o.y = pk2(gelu2(rs * __builtin_elementwise_fma(nm, hi2(c0), hi2(v0))));
          o.z = pk2(gelu2(rs * __builtin_elementwise_fma(nm, lo2(c1), lo2(v1))));
          o.w = pk2(gelu2(rs * __builtin_elementwise_fma(nm, hi2(c1), hi2(v1))));
          st16<ST_AUX>(o, rO, lane * 16 + ((wi * 4 + m) * 28 + zt * 4 + wj * 2 + (h ^ wi)) * 1024);
        }
      }
      STAMP(0, 5);
    } else {
#pragma unroll
      for (int n = 0; n < 4; ++n) bsr[n] = b_sp[pm * 128 + wj * 64 + n * 16 + fr];
      if (w == 0 && lane == 0) { *(LAS u64*)(lds + T_EPO) = epoch; *(LAS unsigned*)(lds + T_EPO + 8) = 0u; }
      asm volatile("s_waitcnt lgkmcnt(0)" : "+v"(bsr[0]), "+v"(bsr[1]), "+v"(bsr[2]), "+v"(bsr[3]) :: "memory");
      tg = next_tags(epoch);
      BAR();
      SCHED();
      STAMP(0, 1);
      p1_loop<2>(lds, acc, w, lane, [&](int k) { STEPSTAMP(k); });
      STAMP(0, 2);
      BAR();
      MEMFENCE();
      f32x2 z[4][2][4];
      {
        RowLN r;
        read_rowln(r, lds, wi, fq);
        f32x2 s1[2][4], s2[2][4];
#pragma unroll
        for (int n = 0; n < 4; ++n) {
          const float c1 = *(LAS float*)(lds + T_COL + (wj * 64 + (n ^ (2 * wi)) * 16 + fr) * 4);
          const f32x2 cc = {c1, c1};
#pragma unroll
          for (int h = 0; h < 2; ++h)
#pragma unroll
            for (int e = 0; e < 4; ++e) {
              const f32x2 g = gelu2(ln_val(r, acc, n, h, e, cc));
              z[n][h][e] = g;
              if (n == 0) { s1[h][e] = g; s2[h][e] = g * g; } else { s1[h][e] += g; s2[h][e] = __builtin_elementwise_fma(g, g, s2[h][e]); }
            }
        }
#pragma unroll
        for (int h = 0; h < 2; ++h) {
          const float v[16] = {s1[h][0].x, s2[h][0].x, s1[h][0].y, s2[h][0].y, s1[h][1].x, s2[h][1].x, s1[h][1].y, s2[h][1].y,
                               s1[h][2].x, s2[h][2].x, s1[h][2].y, s2[h][2].y, s1[h][3].x, s2[h][3].x, s1[h][3].y, s2[h][3].y};
          *(LAS float*)(lds + T_PART + ((wj * 128 + wi * 64 + 32 * h + 8 * fq) * 2 + fr) * 4) = reduce_scatter16(v, fr);
        }
      }
      asm volatile("s_waitcnt lgkmcnt(0)" ::: "memory");
      BAR();
      MEMFENCE();
      if (w < 2) {
        const int row = w * 64 + lane;
        const f32x2 p = *(LAS f32x2*)(lds + T_PART + row * 8) + *(LAS f32x2*)(lds + T_PART + (128 + row) * 8);
        st16<ST_AUX>((u32x4){__float_as_uint(p.x), tg.a, __float_as_uint(p.y), tg.b}, mk_rsrc(ws + WS_STAT + (size_t)pm * 7 * 2048, 7 * 2048), row * 16 + pn * 2048);
      }
      STAMP(0, 3);
      BAR();
      MEMFENCE();
      STAMP(0, 4);
      const rsrc_t rO = mk_rsrc(ws + WS_CT + (size_t)c * 8 * 65536 + pm * 4096, 8 * 65536);
#pragma unroll
      for (int h = 0; h < 2; ++h) {
        const int ro = T_FIN + (wi * 64 + 32 * h + 8 * fq) * 4;
        const f32x4 m0 = *(LAS f32x4*)(lds + ro), m1 = *(LAS f32x4*)(lds + ro + 16), r0 = *(LAS f32x4*)(lds + ro + 512), r1 = *(LAS f32x4*)(lds + ro + 528);
        const f32x2 nm[4] = {lo2(m0), hi2(m0), lo2(m1), hi2(m1)}, rs[4] = {lo2(r0), hi2(r0), lo2(r1), hi2(r1)};
#pragma unroll
        for (int n = 0; n < 4; ++n) {
          u32x4 o;
#pragma unroll
          for (int e = 0; e < 4; ++e) o[e] = pk2((z[n][h][e] + nm[e]) * rs[e]);
          st16<ST_AUX>(o, rO, lane * 16 + (wj * 4 + (n ^ (2 * wi))) * 65536 + (wi * 2 + h) * 1024);
        }
      }
      STAMP(0, 5);
      publish_flag(ws, c, pm, w, lane, tg);
      STAMP(0, 6);
    }
    p2_loop(lds, acc, w, lane, ws, [&](int it) { if (it == 0 && c < 7) publish_flag(ws, 15 + c, pm, w, lane, tg); });
    STAMP(1, 2);
    if (isY) {
      const rsrc_t rO = mk_rsrc(ws + WS_Y + ((size_t)type * 128 + pm * 8) * 16 * 1024, 8 * 16 * 1024);
#pragma unroll
      for (int n = 0; n < 4; ++n)
#pragma unroll
        for (int h = 0; h < 2; ++h) {
          const f32x4 v0 = acc[2 * h][n], v1 = acc[2 * h + 1][n];
          const u32x4 o = {pk2(lo2(v0)), pk2(hi2(v0)), pk2(lo2(v1)), pk2(hi2(v1))};
          st16<ST_AUX>(o, rO, lane * 16 + ((wj * 4 + n) * 16 + pn * 4 + wi * 2 + h) * 1024);
        }
    } else {
      const rsrc_t rO = mk_rsrc(ws + WS_H + (size_t)pm * 8 * 28 * 1024, 8 * 28 * 1024);
#pragma unroll
      for (int n = 0; n < 4; ++n) {
        const float b1 = bflo(bsr[n]);
        const f32x2 b = {b1, b1};
#pragma unroll
        for (int h = 0; h < 2; ++h) {
          const f32x4 v0 = acc[2 * h][n], v1 = acc[2 * h + 1][n];
          const u32x4 zz = *(LAS u32x4*)(lds + A_TAB + ((wj * 4 + n) * 4 + wi * 2 + h) * 1024 + lane * 16);
          u32x4 o;
          o.x = pk2(up2(zz.x) * (lo2(v0) + b));
          o.y = pk2(up2(zz.y) * (hi2(v0) + b));
          o.z = pk2(up2(zz.z) * (lo2(v1) + b));
          o.w = pk2(up2(zz.w) * (hi2(v1) + b));
          st16<ST_AUX>(o, rO, lane * 16 + ((wj * 4 + n) * 28 + pn * 4 + wi * 2 + h) * 1024);
        }
      }
    }
    STAMP(1, 5);
  }
  asm volatile("s_waitcnt lgkmcnt(0)" ::: "memory");
  BAR();
  MEMFENCE();
  if (!spare) publish_at(ws + WS_FL3 + (size_t)((pm * 2 + wj) * 32 + (isY ? 16 + c * 2 + wi : pn * 2 + wi)) * 8, lane, tg);
  STAMP(1, 6);
  {
    const unsigned lb = (unsigned)(size_t)lds;
    const int tt0 = pm * 8 + hb * 4;
    u32x2 xv[4];
#pragma unroll
    for (int n = 0; n < 4; ++n) xv[n] = *(const u32x2*)(x + (size_t)((tt0 + n) * 16 + fr) * D_ + on * 64 + w * 16 + 4 * fq);
    u32x2 wf[28][2];
    BAR();
    SCHED();
    {
      const int row = 4 * fq + (fr >> 2);
      const unsigned a0 = lb + row * 128 + ((w ^ ((row >> 1) & 3)) << 5) + (fr & 3) * 8, a1 = a0 + 57344;
      static_for<28>([&](auto S) {
        constexpr int s = S.value;
        dstr64<(s % 14) * 4096>(wf[s][0], s < 14 ? a0 : a1);
        dstr64<(s % 14) * 4096 + 2048>(wf[s][1], s < 14 ? a0 : a1);
        if constexpr ((s & 3) == 3) WAIT_L0();
      });
    }
    BAR();
    MEMFENCE();
    SCHED();
    u32x2 yr[4], yi[4];
    f32x4 oc[4];
#pragma unroll
    for (int n = 0; n < 4; ++n) oc[n] = (f32x4){0.f, 0.f, 0.f, 0.f};
    const unsigned h0 = lb + lane * 16, h1 = h0 + 65536;
    bf16x8 qa[8], qb[8];
    auto rdq = [&](bf16x8 (&q)[8], auto I, auto HC) {
      constexpr int i = I.value, hc = HC.value, ch = hc >> 1;
      ds128<(ch & 3) * 16384 + (i & 3) * 4096 + (2 * (hc & 1) + (i >> 2)) * 1024>(q[i], ch < 4 ? h0 : h1);
    };
    auto halfc = [&](const bf16x8 (&q)[8], bf16x8 (&g)[8], auto HC) {
      constexpr int hc = HC.value;
      static_for<8>([&](auto I) {
        constexpr int i = I.value, s = 2 * hc + (i >> 2);
        if constexpr (hc + 1 < 14) rdq(g, I, IC<hc + 1>{});
        asm volatile("s_waitcnt lgkmcnt(%0)" ::"n"(hc + 1 < 14 ? 8 : 7 - i) : "memory");
        SCHED();
        oc[i & 3] = MFMA(join(wf[s][0], wf[s][1]), q[i], oc[i & 3]);
        SCHED();
      });
    };
    SCHED();
    BAR();
    SCHED();
    STAMP(2, 1);
    static_for<8>([&](auto I) { rdq(qa, I, IC<0>{}); });
    static_for<7>([&](auto CH) {
      constexpr int ch = CH.value;
      halfc(qa, qb, IC<2 * ch>{});
      if constexpr (ch < 6) { BAR(); SCHED(); }
      if constexpr (ch == 3) {
        MEMFENCE();
        const rsrc_t rY = mk_rsrc(ws + WS_Y, 2 * 128 * 16 * 1024);
#pragma unroll
        for (int n = 0; n < 4; ++n) {
          yr[n] = __builtin_amdgcn_raw_buffer_load_b64(rY, lane * 16 + wj * 8, ((tt0 + n) * 16 + on * 2 + wi) * 1024, 16);
          yi[n] = __builtin_amdgcn_raw_buffer_load_b64(rY, lane * 16 + wj * 8, ((128 + tt0 + n) * 16 + on * 2 + wi) * 1024, 16);
        }
        SCHED();
      }
      halfc(qb, qa, IC<2 * ch + 1>{});
    });
    STAMP(2, 2);
#pragma unroll
    for (int n = 0; n < 4; ++n) {
      u32x2 o;
      o.x = pk2(up2(xv[n].x) + (up2(yr[n].x) - up2(yi[n].x)) + lo2(oc[n]));
      o.y = pk2(up2(xv[n].y) + (up2(yr[n].y) - up2(yi[n].y)) + hi2(oc[n]));
      *(u32x2*)(out + (size_t)((tt0 + n) * 16 + fr) * D_ + on * 64 + w * 16 + 4 * fq) = o;
    }
    STAMP(2, 5);
    STAMP(2, 6);
    asm volatile(".Lfm_e2:");
  }
#endif
}

extern "C" void kernel_launch(void* const* d_in, const int* in_sizes, int n_in, void* d_out, int out_size, void* d_ws,
                              size_t ws_size, hipStream_t stream) {
  const u16* x = (const u16*)d_in[0];
  const u16* ws_re = (const u16*)d_in[1];
  const u16* ws_im = (const u16*)d_in[2];
  const u16* wd_re = (const u16*)d_in[3];
  const u16* wd_im = (const u16*)d_in[4];
  const u16* w_in = (const u16*)d_in[6];
  const u16* w_sp = (const u16*)d_in[7];
  const u16* b_sp = (const u16*)d_in[8];
  const u16* w_out = (const u16*)d_in[9];
  (void)hipFuncSetAttribute((const void*)fm_main, hipFuncAttributeMaxDynamicSharedMemorySize, A_LDS);
  fm_main<<<256, 512, A_LDS, stream>>>(x, wd_re, wd_im, w_in, ws_re, ws_im, w_sp, b_sp, w_out, (char*)d_ws, (u16*)d_out);
}
```

```cpp
#include <hip/hip_runtime.h>
#include <hip/hip_bf16.h>
#include <stdint.h>

typedef unsigned short u16;
typedef __bf16 bf16x8 __attribute__((ext_vector_type(8)));
typedef float f32x4 __attribute__((ext_vector_type(4)));
typedef float f32x2 __attribute__((ext_vector_type(2)));
typedef unsigned u32x4 __attribute__((ext_vector_type(4)));
typedef unsigned u32x2 __attribute__((ext_vector_type(2)));
typedef unsigned long long u64;
#define LAS __attribute__((address_space(3)))
#define GAS __attribute__((address_space(1)))
typedef __amdgpu_buffer_rsrc_t rsrc_t;

constexpr int S_ = 2048, D_ = 512, F_ = 1792, FH = 896;

constexpr size_t WS_EPOCH = 0;
constexpr size_t WS_STAT = 4096;
constexpr size_t WS_ROW = 512u << 10;
constexpr size_t WS_COL = 576u << 10;
constexpr int FS = 128;
constexpr size_t WS_FLG = 640u << 10;
constexpr size_t WS_FL3 = 832u << 10;
constexpr size_t WS_CT = 1u << 20;
constexpr size_t WS_Z1 = WS_CT + (size_t)120 * 65536;
constexpr size_t WS_H = WS_Z1 + (size_t)128 * 28 * 1024;
constexpr size_t WS_Y = WS_H + (size_t)128 * 28 * 1024;

#define ST_AUX 16

template <int V> struct IC { static constexpr int value = V; };
template <int N, int I = 0, class F> __device__ __forceinline__ void static_for(F&& f) {
  if constexpr (I < N) { f(IC<I>{}); static_for<N, I + 1>(f); }
}

__device__ __forceinline__ float bflo(unsigned w) { return __uint_as_float(w << 16); }
__device__ __forceinline__ float bfhi(unsigned w) { return __uint_as_float(w & 0xffff0000u); }
__device__ __forceinline__ unsigned pk_bf16(float lo, float hi) {
  typedef __bf16 bf16x2 __attribute__((ext_vector_type(2)));
  f32x2 v = {lo, hi};
  return __builtin_bit_cast(unsigned, __builtin_convertvector(v, bf16x2));
}
__device__ __forceinline__ f32x2 gelu2(f32x2 x) {
  const float c1 = -2.0f * 0.7978845608028654f * 1.4426950408889634f;
  const float c3 = c1 * 0.044715f;
  const f32x2 t = x * __builtin_elementwise_fma(x * x, (f32x2){c3, c3}, (f32x2){c1, c1});
  f32x2 e = {__builtin_amdgcn_exp2f(t.x), __builtin_amdgcn_exp2f(t.y)};
  e = e + (f32x2){1.0f, 1.0f};
  return x * (f32x2){__builtin_amdgcn_rcpf(e.x), __builtin_amdgcn_rcpf(e.y)};
}
__device__ __forceinline__ f32x2 lo2(f32x4 v) { return (f32x2){v.x, v.y}; }
__device__ __forceinline__ f32x2 hi2(f32x4 v) { return (f32x2){v.z, v.w}; }
__device__ __forceinline__ unsigned pk2(f32x2 v) { return pk_bf16(v.x, v.y); }
__device__ __forceinline__ f32x2 up2(unsigned w) { return (f32x2){bflo(w), bfhi(w)}; }
__device__ __forceinline__ rsrc_t mk_rsrc(const void* p, unsigned bytes) {
  return __builtin_amdgcn_make_buffer_rsrc((void*)p, (short)0, (int)bytes, 0x00020000);
}
#define WAIT_V(n) asm volatile("s_waitcnt vmcnt(%0)" ::"n"(n) : "memory")
#define BAR() __builtin_amdgcn_s_barrier()
#define SCHED() __builtin_amdgcn_sched_barrier(0)
#define MEMFENCE() asm volatile("" ::: "memory")
#define WAIT_L0() do { asm volatile("s_waitcnt lgkmcnt(0)" ::: "memory"); SCHED(); } while (0)
#define DMA16(rs, ldsp, voff, soff) __builtin_amdgcn_raw_ptr_buffer_load_lds(rs, (LAS void*)(ldsp), 16, voff, soff, 0, 0)
#define DMA16D(rs, ldsp, voff, soff) __builtin_amdgcn_raw_ptr_buffer_load_lds(rs, (LAS void*)(ldsp), 16, voff, soff, 0, 16)
#define TOUCH(rs, ldsp, voff, soff) __builtin_amdgcn_raw_ptr_buffer_load_lds(rs, (LAS void*)(ldsp), 4, voff, soff, 0, 0)
#define MFMA(a, b, c) __builtin_amdgcn_mfma_f32_16x16x32_bf16(a, b, c, 0, 0, 0)

template <int AUX> __device__ __forceinline__ void st16(u32x4 v, rsrc_t r, int off) { __builtin_amdgcn_raw_buffer_store_b128(v, r, off, 0, AUX); }
template <int OFF> __device__ __forceinline__ void ds128(bf16x8& d, unsigned a) {
  asm volatile("ds_read_b128 %0, %1 offset:%2" : "=v"(d) : "v"(a), "n"(OFF));
}
template <int OFF> __device__ __forceinline__ void dstr64(u32x2& d, unsigned a) {
  asm volatile("ds_read_b64_tr_b16 %0, %1 offset:%2" : "=v"(d) : "v"(a), "n"(OFF));
}
__device__ __forceinline__ bf16x8 join(const u32x2& lo, const u32x2& hi) {
  return __builtin_bit_cast(bf16x8, (u32x4){lo.x, lo.y, hi.x, hi.y});
}
__device__ __forceinline__ float dpp_add16(float v) {
  v += __builtin_bit_cast(float, __builtin_amdgcn_update_dpp(0, __builtin_bit_cast(int, v), 0x128, 0xf, 0xf, false));
  v += __builtin_bit_cast(float, __builtin_amdgcn_update_dpp(0, __builtin_bit_cast(int, v), 0x124, 0xf, 0xf, false));
  v += __builtin_bit_cast(float, __builtin_amdgcn_update_dpp(0, __builtin_bit_cast(int, v), 0x122, 0xf, 0xf, false));
  v += __builtin_bit_cast(float, __builtin_amdgcn_update_dpp(0, __builtin_bit_cast(int, v), 0x121, 0xf, 0xf, false));
  return v;
}

template <int CTRL> __device__ __forceinline__ float dpp_mov(float v) {
  return __builtin_bit_cast(float, __builtin_amdgcn_update_dpp(0, __builtin_bit_cast(int, v), CTRL, 0xf, 0xf, false));
}
__device__ __forceinline__ float reduce_scatter16(const float (&v)[16], int fr) {
  float a[8], b[4], c[2];
  const bool b3 = fr & 8, b2 = fr & 4, b1 = fr & 2, b0 = fr & 1;
#pragma unroll
  for (int i = 0; i < 8; ++i) a[i] = (b3 ? v[i + 8] : v[i]) + dpp_mov<0x128>(b3 ? v[i] : v[i + 8]);
#pragma unroll
  for (int i = 0; i < 4; ++i) b[i] = (b2 ? a[i + 4] : a[i]) + dpp_mov<0x141>(b2 ? a[i] : a[i + 4]);
#pragma unroll
  for (int i = 0; i < 2; ++i) c[i] = (b1 ? b[i + 2] : b[i]) + dpp_mov<0x4E>(b1 ? b[i] : b[i + 2]);
  return (b0 ? c[1] : c[0]) + dpp_mov<0xB1>(b0 ? c[0] : c[1]);
}

constexpr int A_TAB = 131072;
constexpr int T_ROW = A_TAB, T_COL = A_TAB + 1024, T_PART = A_TAB + 2048, T_FIN = A_TAB + 4096, T_EPO = A_TAB + 6144, T_JUNK = A_TAB + 7168;
constexpr int A_LDS = A_TAB + 32768;
constexpr int SPIN = 1 << 20;
constexpr int KPUB = 2;
constexpr int ZU = 3;
constexpr int SDELAY = 16;
constexpr int FDELAY = 24;

struct Tags { unsigned a, b; };
__device__ __forceinline__ Tags next_tags(u64 e) { return Tags{(unsigned)e + 1u, (unsigned)(e >> 32) + 0x9E3779B9u}; }
__device__ __forceinline__ u64 tag64(Tags t) { return ((u64)t.b << 32) | t.a; }
__device__ __forceinline__ void ld16_issue(u32x4& g, const char* p) { asm volatile("global_load_dwordx4 %0, %1, off sc1" : "=&v"(g) : "v"(p) : "memory"); }
__device__ __forceinline__ u32x4 ld16_now(const char* p) {
  u32x4 g;
  asm volatile("global_load_dwordx4 %0, %1, off sc1\n\ts_waitcnt vmcnt(0)" : "=&v"(g) : "v"(p) : "memory");
  return g;
}
__device__ __forceinline__ u32x2 ld8_now(const char* p) {
  u32x2 g;
  asm volatile("global_load_dwordx2 %0, %1, off sc1\n\ts_waitcnt vmcnt(0)" : "=&v"(g) : "v"(p) : "memory");
  return g;
}
__device__ __forceinline__ int korder32(int t, int j, int vl) {
  if (t < 1) return j;
  const int t1 = t - 1;
  if (t1 < 28) { const int u = t1 >> 2, v = t1 & 3; return v == vl ? 4 * (u + 1) + j : 4 * u + ((j + 1 + (v < vl ? v : v - 1)) & 3); }
  return 28 + ((j + 1 + (t1 - 28)) & 3);
}
__device__ __forceinline__ float row4_sum(const u32x4 (&v)[4]) {
  f32x2 s = {0.f, 0.f};
#pragma unroll
  for (int i = 0; i < 4; ++i) s += (up2(v[i].x) + up2(v[i].y)) + (up2(v[i].z) + up2(v[i].w));
  return dpp_add16(s.x + s.y);
}

template <int MODE, class Hook>
__device__ __forceinline__ void p1_loop(LAS char* lds, f32x4 (&acc)[4][4], int w, int lane, Hook&& hook) {
  const int wi = w >> 1, wj = w & 1, fr = lane & 15, fq = lane >> 4;
  const unsigned lb = (unsigned)(size_t)lds;
  constexpr int NAW = MODE == 0 ? 2 : 4;
  unsigned aX[2], aW[NAW];
#pragma unroll
  for (int ks = 0; ks < 2; ++ks) {
    aX[ks] = lb + (((wi * 64 + fr) * 128 + ((fq ^ (fr >> 1)) << 4)) ^ (ks * 64));
    if constexpr (MODE == 0) aW[ks] = lb + 16384 + (((wj * 64 + fr) * 128 + ((fq ^ (fr >> 1)) << 4)) ^ (ks * 64));
  }
  if constexpr (MODE != 0) {
    const int tq = fr >> 2, tp = fr & 3, tf = ((tq >> 1) & 1) | ((fq & 1) << 1);
#pragma unroll
    for (int n = 0; n < 4; ++n) aW[n] = lb + 16384 + wj * 8192 + (8 * fq + tq) * 128 + ((n ^ tf) << 5) + tp * 8;
  }
#pragma unroll
  for (int m = 0; m < 4; ++m)
#pragma unroll
    for (int n = 0; n < 4; ++n) acc[m][n] = (f32x4){0.f, 0.f, 0.f, 0.f};
  struct Frag { bf16x8 x[4]; bf16x8 w[MODE == 0 ? 4 : 1]; u32x2 t[MODE == 0 ? 1 : 4][2]; };
  Frag fa, fb;
  constexpr int NR = MODE == 0 ? 8 : 12;
  auto rd = [&](Frag& f, auto I, auto KS) {
    constexpr int i = I.value, ks = KS.value;
    if constexpr (i < 4) ds128<i * 2048>(f.x[i], aX[ks]);
    else if constexpr (MODE == 0) ds128<(i - 4) * 2048>(f.w[i - 4], aW[ks]);
    else { constexpr int n = (i - 4) >> 1, t = (i - 4) & 1; dstr64<(32 * ks + 4 * t) * 128>(f.t[n][t], aW[n]); }
  };
  auto wfrag = [&](const Frag& f, int n) -> bf16x8 { if constexpr (MODE == 0) return f.w[n]; else return join(f.t[n][0], f.t[n][1]); };
  int dS = 32768;
  auto half = [&](const Frag& f, Frag& g, auto KS) {
    static_for<16>([&](auto I) {
      constexpr int i = I.value, n = i >> 2, m = (n & 1) ? 3 - (i & 3) : i & 3;
      if constexpr (MODE == 1) acc[m][n] = MFMA(wfrag(f, n), f.x[m], acc[m][n]);
      else acc[m][n] = MFMA(f.x[m], wfrag(f, n), acc[m][n]);
      SCHED();
      if constexpr (i < NR) { rd(g, I, KS); SCHED(); }
      else if constexpr (i >= 12) {
        if constexpr (KS.value == 1 && i - 12 < NAW) aW[i - 12] += dS;
        if constexpr (KS.value == 1 && i == 15) aX[0] += dS;
        if constexpr (KS.value == 0 && i == 12) aX[1] += dS;
        SCHED();
      }
    });
    WAIT_L0();
  };
#pragma unroll
  for (int m = 0; m < 4; ++m) asm volatile("" : "+v"(acc[m][0]), "+v"(acc[m][1]), "+v"(acc[m][2]), "+v"(acc[m][3]));
  SCHED();
  hook(-1);
  asm volatile("s_waitcnt lgkmcnt(0)" ::: "memory");
  BAR();
  SCHED();
  static_for<NR>([&](auto I) { rd(fa, I, IC<0>{}); });
  WAIT_L0();
#pragma unroll 1
  for (int k = 0; k < 8; ++k) {
    dS = (k & 3) == 3 ? -98304 : 32768;
    half(fa, fb, IC<1>{});
    BAR();
    SCHED();
    half(fb, fa, IC<0>{});
    hook(k);
    SCHED();
  }
}

template <class Hook>
__device__ __forceinline__ void p2_loop(LAS char* lds, f32x4 (&acc)[4][4], int w, int lane, Hook&& hook) {
  const int wi = w >> 1, wj = w & 1, fr = lane & 15, fq = lane >> 4;
  const unsigned lb = (unsigned)(size_t)lds;
  unsigned aP[2], aQ[2][2];
#pragma unroll
  for (int sp = 0; sp < 2; ++sp) {
    aP[sp] = lb + sp * 65536 + wi * 8192 + lane * 16;
#pragma unroll
    for (int ks = 0; ks < 2; ++ks) aQ[sp][ks] = lb + sp * 65536 + 16384 + (((wj * 64 + fr) * 128 + ((fq ^ (fr >> 1)) << 4)) ^ (ks * 64));
  }
#pragma unroll
  for (int m = 0; m < 4; ++m)
#pragma unroll
    for (int n = 0; n < 4; ++n) acc[m][n] = (f32x4){0.f, 0.f, 0.f, 0.f};
  bf16x8 fa[8], fb[8];
  auto rd = [&](bf16x8 (&f)[8], auto I, auto S, auto KS) {
    constexpr int x = I.value, s = S.value, ks = KS.value;
    if constexpr (x < 4) ds128<(s & 1) * 32768 + (x * 2 + ks) * 1024>(f[x], aP[s >> 1]);
    else ds128<(s & 1) * 32768 + (x - 4) * 2048>(f[x], aQ[s >> 1][ks]);
  };
  auto half = [&](const bf16x8 (&f)[8], bf16x8 (&g)[8], auto S, auto KS) {
    static_for<16>([&](auto I) {
      constexpr int i = I.value;
      constexpr int m = i >> 2, n = (m & 1) ? 3 - (i & 3) : i & 3;
      acc[m][n] = MFMA(f[m], f[4 + n], acc[m][n]);
      SCHED();
      if constexpr (i < 8) { rd(g, I, S, KS); SCHED(); }
    });
    WAIT_L0();
  };
#pragma unroll
  for (int m = 0; m < 4; ++m) asm volatile("" : "+v"(acc[m][0]), "+v"(acc[m][1]), "+v"(acc[m][2]), "+v"(acc[m][3]));
  BAR();
  SCHED();
  static_for<8>([&](auto I) { rd(fa, I, IC<0>{}, IC<0>{}); });
  WAIT_L0();
#pragma unroll 1
  for (int it = 0; it < 8; ++it) {
    static_for<4>([&](auto S) {
      half(fa, fb, S, IC<1>{});
      if (S.value < 3 || it < 7) BAR();
      SCHED();
      half(fb, fa, IC<(S.value + 1) & 3>{}, IC<0>{});
    });
    hook(it);
    SCHED();
  }
}

struct RowLN { f32x2 nm[2][4], rs[2][4]; };
__device__ __forceinline__ void read_rowln(RowLN& r, LAS char* lds, int wi, int fq) {
#pragma unroll
  for (int h = 0; h < 2; ++h)
#pragma unroll
    for (int e = 0; e < 4; ++e) {
      const f32x4 t = *(LAS f32x4*)(lds + T_ROW + (wi * 64 + 32 * h + 8 * fq + 2 * e) * 8);
      r.nm[h][e] = (f32x2){-t.x, -t.z}; r.rs[h][e] = (f32x2){t.y, t.w};
    }
}
__device__ __forceinline__ f32x2 ln_val(const RowLN& r, const f32x4 (&acc)[4][4], int n, int h, int e, f32x2 c) {
  const f32x4 v = acc[2 * h + (e >> 1)][n];
  return r.rs[h][e] * __builtin_elementwise_fma(r.nm[h][e], c, (e & 1) ? hi2(v) : lo2(v));
}
__device__ __forceinline__ void publish_at(char* p, int lane, Tags tg) {
  WAIT_V(0);
  if (lane == 0) __hip_atomic_store((GAS u64*)p, tag64(tg), __ATOMIC_RELAXED, __HIP_MEMORY_SCOPE_AGENT);
}
__device__ __forceinline__ void publish_flag(char* ws, int c, int pm, int w, int lane, Tags tg) {
  publish_at(ws + WS_FLG + (size_t)(c * 64 + pm * 4 + w) * FS, lane, tg);
}
__device__ __forceinline__ void wait_flags(const char* fp, Tags tg) {
#pragma unroll 1
  for (int spin = 0; spin < SPIN; ++spin) {
    const u32x2 fl = ld8_now(fp);
    if (__all(fl.x == tg.a && fl.y == tg.b)) break;
    __builtin_amdgcn_s_sleep(1);
  }
}

struct Ring2 {
  rsrc_t rQ, rP; unsigned voffQ, voffP; LAS char* lw_lds; int j, vl, kr;
  __device__ __forceinline__ void Q(int buf, int kk) const {
#pragma unroll
    for (int i = 0; i < 4; ++i) DMA16(rQ, lw_lds + buf * 32768 + 16384 + i * 4096, voffQ, kk * 128 + i * 32 * S_ * 2);
  }
  __device__ __forceinline__ void P(int buf, int kk) const {
#pragma unroll
    for (int i = 0; i < 4; ++i) DMA16D(rP, lw_lds + buf * 32768 + i * 4096, voffP, kk * 2048 + i * 131072);
  }
  __device__ __forceinline__ int ord(int t) const { return (korder32(t, j, vl) + kr) & 31; }
};

template <bool G>
__device__ __forceinline__ Tags p1_loader(LAS char* lds, char* ws, const u16* x, const u16* wd, const u16* win, const Ring2& r2,
                                          int k0, int pm, int pn, int c, int lw, int lane) {
  constexpr int NK = G ? 8 : 16;
  const bool noz = !G && c == 7;
  const int rho = 8 * lw + (lane >> 3);
  const unsigned sw = (unsigned)(((lane & 7) ^ ((rho >> 1) & 7)) << 4);
  const rsrc_t rX = mk_rsrc(x + (size_t)pm * 128 * D_, 128 * D_ * 2);
  const unsigned voffXp = (unsigned)((8 * ((rho >> 2) & 3) + 4 * (rho >> 4) + (rho & 3)) * (D_ * 2)) + sw;
  const unsigned voffXn = (unsigned)(rho * (D_ * 2)) + sw;
  const rsrc_t rW = mk_rsrc(win, noz ? 0 : (D_ - 1) * F_ * 2 + 256), rD = mk_rsrc(wd, 128 * D_ * 2), rX2 = mk_rsrc(x + (size_t)pm * 128 * D_, noz ? 0 : 128 * D_ * 2);
  const int f = ((rho >> 1) & 1) | (((rho >> 3) & 1) << 1);
  const unsigned voffW = (unsigned)(rho * (F_ * 2) + (((((lane & 7) >> 1) ^ f) << 5) | ((lane & 1) << 4)));
  LAS char* lw_lds = lds + lw * 1024;
  auto p1part = [&](auto H) {
    constexpr int h = H.value, t = h >> 1;
    const int kk = (t + k0) & 7;
    LAS char* p = lw_lds + (t & 3) * 32768;
    if constexpr ((h & 1) == 0) {
#pragma unroll
      for (int i = 0; i < 4; ++i) DMA16(t < 8 ? rX : rX2, p + i * 4096, t < 8 ? voffXp : voffXn, kk * 128 + i * 32 * D_ * 2);
    } else if constexpr (!G && t < 8) {
#pragma unroll
      for (int i = 0; i < 4; ++i) DMA16(rD, p + 16384 + i * 4096, voffXn, kk * 128 + i * 32 * D_ * 2);
    } else {
#pragma unroll
      for (int i = 0; i < 4; ++i) DMA16(rW, p + 16384 + i * 4096, voffW, (kk * 64 + (i & 1) * 32) * (F_ * 2) + (i >> 1) * 128);
    }
  };
  constexpr int BG = G ? 6 : 5;
  constexpr auto upto = [](int b) constexpr { int h = 3 + 3 * b, lim = b == 0 ? 8 : 2 * (b + 3); h = h < lim ? h : lim; return h < 2 * NK ? h : 2 * NK; };
  constexpr auto after = [upto](int hx, int b) constexpr {
    int n = 0;
    for (int s = 0; s <= b; ++s) {
      for (int h = s ? upto(s - 1) : 0; h < upto(s); ++h) n = h == hx ? 0 : n + 4;
      n += s == BG || s == 13 ? 1 : 0;
      if (!G && s >= 14) n += 4;
    }
    return n;
  };
  const int row = (lw & 1) * 64 + lane;
  const char* gp = ws + (lw < 2 ? WS_ROW + (size_t)(pm * 128 + row) * 16 : WS_COL + (size_t)((G ? 15 + pn : c) * 64 + lane) * 16);
  const char* gp2 = ws + WS_COL + (size_t)((8 + (noz ? 0 : c)) * 64 + lane) * 16;
  u32x4 g;
  Tags tg = {0u, 0u};
  auto table = [&](const char* p, bool mine) {
    SCHED();
    asm volatile("" : "+v"(g));
    if (mine) {
#pragma unroll 1
      for (int spin = 0; spin < SPIN; ++spin) {
        if (__all(g.y == tg.a && g.w == tg.b)) break;
        __builtin_amdgcn_s_sleep(2);
        g = ld16_now(p);
      }
      *(LAS f32x2*)(lds + (lw < 2 ? T_ROW + row * 8 : T_COL + lane * 8)) = (f32x2){__uint_as_float(g.x), __uint_as_float(g.z)};
      asm volatile("s_waitcnt lgkmcnt(0)" ::: "memory");
    }
  };
  static_for<NK>([&](auto B) {
    constexpr int b = B.value, h0 = b ? upto(b - 1) : 3, h1 = upto(b);
    static_for<h1 - h0>([&](auto H) { p1part(IC<h0 + H.value>{}); });
    if constexpr (b == BG) ld16_issue(g, gp);
    if constexpr (!G && b == 9) {
      WAIT_V(32);
      tg = next_tags(*(LAS u64*)(lds + T_EPO));
      table(gp, lw < 3);
      BAR();
      WAIT_V(after(17, 9));
      BAR();
    }
    if constexpr (b == 13) ld16_issue(g, gp2);
    if constexpr (!G && b >= 14) r2.Q(b - 14, r2.ord(b - 14));
    if constexpr (!(!G && b == 8)) WAIT_V(after(2 * b + 1, b));
    BAR();
    MEMFENCE();
  });
  if constexpr (G) {
    BAR();
    WAIT_V(0);
    tg = next_tags(*(LAS u64*)(lds + T_EPO));
    table(gp, lw < 3);
    BAR();
    r2.Q(0, r2.ord(0)); r2.Q(1, r2.ord(1)); r2.Q(2, r2.ord(2)); r2.Q(3, r2.ord(3));
    BAR();
    const int r2w = lw * 32 + (lane & 31), h2 = lane >> 5;
    const char* sp = ws + WS_STAT + (size_t)pm * 7 * 2048 + h2 * 3 * 2048 + r2w * 16;
    u32x4 q[4];
    __builtin_amdgcn_s_sleep(SDELAY);
#pragma unroll 1
    for (int spin = 0; spin < SPIN; ++spin) {
#pragma unroll
      for (int i = 0; i < 4; ++i) ld16_issue(q[i], sp + i * 2048);
      WAIT_V(0);
      SCHED();
      asm volatile("" : "+v"(q[0]), "+v"(q[1]), "+v"(q[2]), "+v"(q[3]));
      bool ok = true;
#pragma unroll
      for (int i = 0; i < 4; ++i) ok = ok && q[i].y == tg.a && q[i].w == tg.b;
      if (__all(ok)) break;
    }
    float t1 = __uint_as_float(q[1].x) + __uint_as_float(q[2].x) + __uint_as_float(q[3].x) + (h2 ? 0.f : __uint_as_float(q[0].x));
    float t2 = __uint_as_float(q[1].z) + __uint_as_float(q[2].z) + __uint_as_float(q[3].z) + (h2 ? 0.f : __uint_as_float(q[0].z));
    t1 += __shfl_xor(t1, 32); t2 += __shfl_xor(t2, 32);
    const float mu = t1 * (1.0f / FH);
    const float rstd = __builtin_amdgcn_rsqf(fmaxf(t2 * (1.0f / FH) - mu * mu, 0.f) + 1e-5f);
    *(LAS float*)(lds + T_FIN + h2 * 512 + r2w * 4) = h2 ? rstd : -mu;
    asm volatile("s_waitcnt lgkmcnt(0)" ::: "memory");
    BAR();
    __builtin_amdgcn_s_sleep(FDELAY);
  } else {
    WAIT_V(8);
    table(gp2, lw == 2);
    r2.Q(2, r2.ord(2));
    BAR();
    r2.Q(3, r2.ord(3));
  }
  return tg;
}

__global__ __launch_bounds__(512, 2) void fm_main(const u16* __restrict__ x, const u16* __restrict__ wd_re,
                                                  const u16* __restrict__ wd_im, const u16* __restrict__ w_in,
                                                  const u16* __restrict__ ws_re, const u16* __restrict__ ws_im,
                                                  const u16* __restrict__ w_sp, const u16* __restrict__ b_sp,
                                                  const u16* __restrict__ w_out, char* ws, u16* __restrict__ out) {
#if defined(__HIP_DEVICE_COMPILE__)
  extern __shared__ __attribute__((aligned(16))) char smem[];
  LAS char* lds = (LAS char*)smem;
  const int w = __builtin_amdgcn_readfirstlane(threadIdx.x >> 6), lane = threadIdx.x & 63;
  const int xcd = blockIdx.x & 7, idx = blockIdx.x >> 3;
  const int wi = (w >> 1) & 1, wj = w & 1, fr = lane & 15, fq = lane >> 4;
  {
    u64 pc; int len1, len2;
    asm volatile("s_getpc_b64 %0\n.Lfm_pc:\n\ts_mov_b32 %1, .Lfm_e1-.Lfm_pc\n\ts_mov_b32 %2, .Lfm_e2-.Lfm_pc" : "=s"(pc), "=s"(len1), "=s"(len2));
    const unsigned len = (unsigned)(len1 > len2 ? len1 : len2);
    if (w == 3 && len < 65536u && (unsigned)idx * 8192u < len) TOUCH(mk_rsrc((const void*)pc, len), lds + T_JUNK, lane * 128, idx * 8192);
  }
  asm volatile("" :: "s"(x), "s"(wd_re), "s"(wd_im), "s"(w_in), "s"(ws_re), "s"(ws_im), "s"(w_sp), "s"(b_sp), "s"(w_out), "s"(ws), "s"(out));
  const bool isY = xcd < 4;
  const int type = xcd >> 1;
  const int lm = isY ? idx >> 2 : idx >> 3;
  const int pm = (isY ? (xcd & 1) * 8 : (xcd - 4) * 4) + lm;
  const int pn = idx & (isY ? 3 : 7);
  const bool spare = pn == 7;
  const int c = (isY ? type * 4 : 8) + pn;
  const int zt = !isY ? pn : c < 7 ? c : 6;
  const int hb = isY ? 1 : 0, on = isY ? c : pn;
  const int k0 = (isY ? lm + 2 * pn : 2 * lm + pn) & 7;
  if (__builtin_expect(w >= 4 && !spare, 1)) {
    const int rho = 8 * (w - 4) + (lane >> 3);
    const unsigned sw = (unsigned)(((lane & 7) ^ ((rho >> 1) & 7)) << 4);
    const rsrc_t rX = mk_rsrc(x + (size_t)pm * 128 * D_, 128 * D_ * 2);
    const unsigned voffX = (unsigned)((8 * ((rho >> 2) & 3) + 4 * (rho >> 4) + (rho & 3)) * (D_ * 2)) + sw;
    const rsrc_t rW = mk_rsrc(isY ? (type ? wd_im : wd_re) + (size_t)pn * 128 * D_ : w_in + FH + pn * 128, isY ? 128 * D_ * 2 : (D_ - 1) * F_ * 2 + 256);
    const int f = ((rho >> 1) & 1) | (((rho >> 3) & 1) << 1);
    const unsigned voffW = isY ? (unsigned)(rho * (D_ * 2)) + sw : (unsigned)(rho * (F_ * 2) + (((((lane & 7) >> 1) ^ f) << 5) | ((lane & 1) << 4)));
    const int wb = k0 * (isY ? 128 : 64 * F_ * 2), w1 = isY ? 32 * D_ * 2 : 32 * F_ * 2, w2 = isY ? 64 * D_ * 2 : 128;
    LAS char* p = lds + (w - 4) * 1024;
#pragma unroll
    for (int i = 0; i < 4; ++i) DMA16(rX, p + i * 4096, voffX, k0 * 128 + i * 32 * D_ * 2);
#pragma unroll
    for (int i = 0; i < 4; ++i) DMA16(rW, p + 16384 + i * 4096, voffW, wb + (i & 1) * w1 + (i >> 1) * w2);
#pragma unroll
    for (int i = 0; i < 4; ++i) DMA16(rX, p + 32768 + i * 4096, voffX, ((k0 + 1) & 7) * 128 + i * 32 * D_ * 2);
  }
  u32x2 ep;
  u32x4 xr[4];
  unsigned bsr[4];
  const int gid = pm * 7 + pn;
  const int row0 = (isY ? xcd * 32 + idx : gid & 63) * 16 + w * 4;
  if (__builtin_expect(w < 4, 1)) {
    const u16* rp = (isY ? x : row0 < 512 ? wd_re : wd_im - 512 * D_) + (size_t)(row0 + fq) * D_ + fr * 8;
    const u16* bp = b_sp + pm * 128 + wj * 64 + fr;
    asm volatile("global_load_dwordx2 %0, %9, off sc1\n\t"
                 "global_load_dwordx4 %1, %10, off\n\tglobal_load_dwordx4 %2, %10, off offset:256\n\t"
                 "global_load_dwordx4 %3, %10, off offset:512\n\tglobal_load_dwordx4 %4, %10, off offset:768\n\t"
                 "global_load_ushort %5, %11, off\n\tglobal_load_ushort %6, %11, off offset:32\n\t"
                 "global_load_ushort %7, %11, off offset:64\n\tglobal_load_ushort %8, %11, off offset:96"
                 : "=&v"(ep), "=&v"(xr[0]), "=&v"(xr[1]), "=&v"(xr[2]), "=&v"(xr[3]), "=&v"(bsr[0]), "=&v"(bsr[1]), "=&v"(bsr[2]), "=&v"(bsr[3])
                 : "v"(ws + WS_EPOCH), "v"(rp), "v"(bp) : "memory");
  }
  auto early_in = [&]() {
    WAIT_V(0);
    SCHED();
    asm volatile("" : "+v"(ep), "+v"(xr[0]), "+v"(xr[1]), "+v"(xr[2]), "+v"(xr[3]), "+v"(bsr[0]), "+v"(bsr[1]), "+v"(bsr[2]), "+v"(bsr[3]));
    if (w == 0 && lane == 0) { *(LAS u32x2*)(lds + T_EPO) = ep; *(LAS unsigned*)(lds + T_EPO + 8) = 0u; }
    return next_tags(((u64)ep.y << 32) | ep.x);
  };
  MEMFENCE();
  Tags tg = {0u, 0u};
  if (spare) {
    const int sid = pm;
    const rsrc_t rA = mk_rsrc(w_in + sid * 64, sid < 14 ? (D_ - 1) * F_ * 2 + FH * 2 + 128 : 0);
    u32x4 v[2][8];
#pragma unroll
    for (int j = 0; j < 2; ++j)
#pragma unroll
      for (int i = 0; i < 8; ++i) v[j][i] = __builtin_amdgcn_raw_buffer_load_b128(rA, (lane >> 3) * (F_ * 2) + (lane & 7) * 16, (w * 64 + i * 8) * (F_ * 2) + (1 - j) * FH * 2, 0);
    if (w < 4) early_in();
#pragma unroll
    for (int j = 0; j < 2; ++j) {
      float s[8];
#pragma unroll
      for (int e = 0; e < 4; ++e) {
        f32x2 t = up2(v[j][0][e]);
#pragma unroll
        for (int i = 1; i < 8; ++i) t += up2(v[j][i][e]);
        s[2 * e] = t.x + dpp_mov<0x128>(t.x); s[2 * e + 1] = t.y + dpp_mov<0x128>(t.y);
      }
      if ((lane & 8) == 0) {
        LAS f32x4* p = (LAS f32x4*)(lds + j * 8192 + (w * 4 + fq) * 256 + (lane & 7) * 32);
        p[0] = (f32x4){s[0], s[1], s[2], s[3]}; p[1] = (f32x4){s[4], s[5], s[6], s[7]};
      }
      asm volatile("s_waitcnt lgkmcnt(0)" ::: "memory");
      BAR();
      MEMFENCE();
      tg = next_tags(*(LAS u64*)(lds + T_EPO));
      if (w == j) {
        f32x2 t = {0.f, 0.f};
#pragma unroll
        for (int i = 0; i < 16; ++i) t += *(LAS f32x2*)(lds + j * 8192 + ((lane >> 5) * 16 + i) * 256 + (lane & 31) * 8);
        t.x += __shfl_xor(t.x, 32); t.y += __shfl_xor(t.y, 32);
        if (lane < 32 && sid < 14) st16<ST_AUX>((u32x4){__float_as_uint(t.x), tg.a, __float_as_uint(t.y), tg.b}, mk_rsrc(ws + WS_COL, 1408 * 16), (512 + (1 - j) * 448 + sid * 32 + lane) * 16);
      }
    }
    if (w < 4) publish_flag(ws, 22, sid, w, lane, tg);
    if (w < 7) TOUCH(mk_rsrc(w_out, FH * D_ * 2), lds + T_JUNK, lane * 128, (sid * 7 + w) * 8192);
    if (sid == 0 && w == 0) {
#pragma unroll 1
      for (int i = 0, spin = 0; i < 16 && spin < SPIN; ++spin) {
        const u32x2 fl = ld8_now(ws + WS_FLG + (size_t)((i ? i - 1 : 22) * 64 + lane) * FS);
        if (__all(fl.x == tg.a && fl.y == tg.b)) ++i; else __builtin_amdgcn_s_sleep(32);
      }
      if (lane == 0) __hip_atomic_store((GAS u64*)(ws + WS_EPOCH), tag64(tg), __ATOMIC_RELAXED, __HIP_MEMORY_SCOPE_AGENT);
    }
    __builtin_amdgcn_s_waitcnt(0x0F70);
    BAR();
  }

  if (w >= 4) {
    const int lw = w - 4, rho = 8 * lw + (lane >> 3);
    LAS char* lw_lds = lds + lw * 1024;
    const rsrc_t rW = mk_rsrc(w_out + on * 64, (FH - 1) * D_ * 2 + 128);
    const unsigned voffW = (unsigned)(rho * (D_ * 2) + (((((lane & 7) >> 1) ^ ((rho >> 1) & 3)) << 5) | ((lane & 1) << 4)));
    const int k3 = on < 7 ? on : 0;
    auto ord3 = [&](int t) { return t + k3 >= 7 ? t + k3 - 7 : t + k3; };
    auto issW = [&](int t0, int t1) {
#pragma unroll 1
      for (int t = t0; t < t1; ++t) {
        const int kk = ord3(t);
#pragma unroll
        for (int i = 0; i < 4; ++i) DMA16(rW, lw_lds + t * 16384 + i * 4096, voffW, (kk * 128 + i * 32) * (D_ * 2));
      }
    };
    if (!spare) {
      Ring2 r2;
      r2.rQ = mk_rsrc((isY ? (type ? ws_im : ws_re) : w_sp) + (size_t)pm * 128 * S_, 128 * S_ * 2);
      r2.rP = mk_rsrc(ws + WS_CT + (size_t)c * 8 * 65536, 8 * 65536);
      r2.voffQ = (unsigned)(rho * (S_ * 2) + (((lane & 7) ^ ((rho >> 1) & 7)) << 4));
      r2.voffP = (unsigned)((lw >> 1) * 65536 + (lw & 1) * 1024 + lane * 16);
      r2.lw_lds = lw_lds;
      r2.j = (lm + pn) & 3;
      r2.kr = 2 * (pm - lm);
      r2.vl = (isY ? (lm >> 2) * 2 + (pn & 1) : (pn >> 2) * 2 + (lm & 1)) & 3;
      if (isY) tg = p1_loader<false>(lds, ws, x, (type ? wd_im : wd_re) + (size_t)pn * 128 * D_, w_in + zt * 128, r2, k0, pm, pn, c, lw, lane);
      else tg = p1_loader<true>(lds, ws, x, wd_re, w_in + FH + pn * 128, r2, k0, pm, pn, c, lw, lane);
      const char* pf = ws + WS_FLG + (size_t)(c * 64 + lane) * FS;
      if (lw == 0) {
        wait_flags(ws + WS_FLG + (size_t)(c * 64 + (pm - lm) * 4 + (lane & 15)) * FS, tg);
        *(volatile LAS unsigned*)(lds + T_EPO + 8) = 1u;
      } else {
#pragma unroll 1
        for (int spin = 0; spin < (SPIN << 4) && __builtin_amdgcn_readfirstlane(*(volatile LAS unsigned*)(lds + T_EPO + 8)) == 0u; ++spin) __builtin_amdgcn_s_sleep(1);
      }
      MEMFENCE();
      r2.P(0, r2.ord(0)); r2.P(1, r2.ord(1));
      WAIT_V(4); BAR();
      r2.P(2, r2.ord(2)); r2.P(3, r2.ord(3));
      u32x2 pfl;
      asm volatile("global_load_dwordx2 %0, %1, off sc1" : "=&v"(pfl) : "v"(pf) : "memory");
      WAIT_V(9); BAR(); MEMFENCE();
      { const int kk = r2.ord(4); r2.P(0, kk); r2.Q(0, kk); }
      WAIT_V(13); BAR(); MEMFENCE();
      WAIT_V(8);
      SCHED();
      asm volatile("" : "+v"(pfl));
      if (!__all(pfl.x == tg.a && pfl.y == tg.b)) wait_flags(pf, tg);
      MEMFENCE();
      int o[4];
#pragma unroll
      for (int v = 0; v < 4; ++v) o[v] = r2.ord(5 + v);
      const rsrc_t rZ = mk_rsrc(ws + WS_Z1 + (size_t)pm * 8 * 28 * 1024, 8 * 28 * 1024);
      const char* zf = ws + WS_FLG + (size_t)((15 + zt) * 64 + pm * 4 + (lane & 3)) * FS;
      int zq = isY ? -1 : ZU * 4 + r2.vl;
#pragma unroll 1
      for (int u = 1; u < 7; ++u) {
        static_for<4>([&](auto V) {
          constexpr int v = V.value;
          r2.P((v + 1) & 3, o[v]);
          if (zq == u * 4 + v) {
            if (u == ZU) {
              __builtin_amdgcn_raw_ptr_buffer_load_lds(mk_rsrc(ws + WS_FLG + (size_t)((15 + zt) * 64 + pm * 4) * FS, 4 * FS), (LAS void*)(lds + A_TAB + 16384 + lw * 256), 4,
                                                       ((lane >> 1) & 3) * FS + (lane & 1) * 4, 0, 0, 16);
              zq += 4;
            }
            else {
              if (u == ZU + 1) {
                const unsigned zv = *(volatile LAS unsigned*)(lds + A_TAB + 16384 + lw * 256 + lane * 4);
                if (!__all(zv == ((lane & 1) ? tg.b : tg.a))) wait_flags(zf, tg);
                zq += 4;
              }
#pragma unroll
              for (int i = 0; i < 4; ++i)
                DMA16D(rZ, lds + A_TAB + (((u - ZU - 1) * 4 + i) * 4 + lw) * 1024, lane * 16, (((u - ZU - 1) * 4 + i) * 28 + pn * 4 + lw) * 1024);
            }
          }
          r2.Q((v + 1) & 3, o[v]);
          o[v] = (o[v] + 4) & 31;
          WAIT_V(16); BAR(); MEMFENCE();
        });
      }
      static_for<3>([&](auto V) {
        const int kk = r2.ord(29 + V.value);
        r2.P((V.value + 1) & 3, kk); r2.Q((V.value + 1) & 3, kk);
        WAIT_V(16); BAR(); MEMFENCE();
      });
      WAIT_V(8); BAR(); MEMFENCE();
      issW(0, 2);
      WAIT_V(8); BAR(); MEMFENCE();
      issW(2, 5);
    } else issW(0, 5);
    BAR();
    MEMFENCE();
    issW(5, 7);
    WAIT_V(0);
    BAR();
    const char* f3 = ws + WS_FL3 + (size_t)((pm * 2 + hb) * 32 + ((lane & 15) < 14 ? lane & 15 : 16 + (lane & 1) * 8 + on)) * FS;
    bool yseen = false;
    if (lw == 0) {
      if (spare) __builtin_amdgcn_s_sleep(127);
#pragma unroll 1
      for (int spin = 0; spin < SPIN; ++spin) {
        const u32x2 fl = ld8_now(f3);
        const u64 m = __ballot(fl.x == tg.a && fl.y == tg.b);
        yseen = (m & 0xC000u) == 0xC000u;
        if ((m & 0x3FFFu) == 0x3FFFu) break;
        __builtin_amdgcn_s_sleep(1);
      }
    }
    BAR();
    MEMFENCE();
    const rsrc_t rH = mk_rsrc(ws + WS_H + (size_t)(pm * 8 + hb * 4 + lw) * 28 * 1024, 28 * 1024);
    static_for<7>([&](auto B) {
      constexpr int b = B.value, s0 = b == 0 ? 0 : b == 1 ? 2 : (b + 2 < 7 ? b + 2 : 7), s1 = b == 0 ? 2 : (b + 3 < 7 ? b + 3 : 7);
      static_for<s1 - s0>([&](auto T) {
        const int kk = ord3(s0 + T.value);
#pragma unroll
        for (int i = 0; i < 4; ++i) DMA16D(rH, lds + (s0 + T.value) * 16384 + lw * 4096 + i * 1024, lane * 16, kk * 4096 + i * 1024);
      });
      if constexpr (b == 4) { if (lw == 0 && !yseen) wait_flags(f3, tg); }
      WAIT_V(4 * (s1 - b - 1));
      BAR();
    });
    asm volatile(".Lfm_e1:");
    return;
  }

  if (!spare) {
    f32x4 acc[4][4];
    if (isY) {
      p1_loop<0>(lds, acc, w, lane, [&](int k) {
        if (k < 0) {
          tg = early_in();
          const float mu = row4_sum(xr) * (1.0f / D_);
          const f32x2 m2 = {mu, mu};
          f32x2 q2 = {0.f, 0.f};
#pragma unroll
          for (int i = 0; i < 4; ++i)
#pragma unroll
            for (int e = 0; e < 4; ++e) { const f32x2 d = up2(xr[i][e]) - m2; q2 = __builtin_elementwise_fma(d, d, q2); }
          const float rstd = __builtin_amdgcn_rsqf(dpp_add16(q2.x + q2.y) * (1.0f / D_) + 1e-5f);
          if (fr == 0) st16<ST_AUX>((u32x4){__float_as_uint(mu), tg.a, __float_as_uint(rstd), tg.b}, mk_rsrc(ws + WS_ROW, S_ * 16), (row0 + fq) * 16);
        }
      });
      BAR();
      MEMFENCE();
      {
        RowLN r;
        read_rowln(r, lds, wi, fq);
        const rsrc_t rO = mk_rsrc(ws + WS_CT + (size_t)c * 8 * 65536 + pm * 4096, 8 * 65536);
#pragma unroll
        for (int n = 0; n < 4; ++n) {
          const float c1 = *(LAS float*)(lds + T_COL + (wj * 64 + n * 16 + fr) * 4);
          const f32x2 cc = {c1, c1};
#pragma unroll
          for (int h = 0; h < 2; ++h) {
            const u32x4 o = {pk2(ln_val(r, acc, n, h, 0, cc)), pk2(ln_val(r, acc, n, h, 1, cc)), pk2(ln_val(r, acc, n, h, 2, cc)), pk2(ln_val(r, acc, n, h, 3, cc))};
            st16<ST_AUX>(o, rO, lane * 16 + (wj * 4 + n) * 65536 + (wi * 2 + h) * 1024);
          }
        }
      }
      if (c == 7) {
        publish_flag(ws, c, pm, w, lane, tg);
#pragma unroll 1
        for (int k = 0; k < 9; ++k) BAR();
      } else {
        p1_loop<1>(lds, acc, w, lane, [&](int k) { if (k == KPUB) publish_flag(ws, c, pm, w, lane, tg); });
        MEMFENCE();
        const rsrc_t rO = mk_rsrc(ws + WS_Z1 + (size_t)pm * 8 * 28 * 1024, 8 * 28 * 1024);
#pragma unroll
        for (int h = 0; h < 2; ++h) {
          const f32x4 c0 = *(LAS f32x4*)(lds + T_COL + (wj * 64 + h * 32 + 4 * fq) * 4);
          const f32x4 c1 = *(LAS f32x4*)(lds + T_COL + (wj * 64 + h * 32 + 16 + 4 * fq) * 4);
#pragma unroll
          for (int m = 0; m < 4; ++m) {
            const f32x2 mr = *(LAS f32x2*)(lds + T_ROW + (wi * 64 + m * 16 + fr) * 8);
            const f32x2 nm = {-mr.x, -mr.x}, rs = {mr.y, mr.y};
            const f32x4 v0 = acc[m][2 * h], v1 = acc[m][2 * h + 1];
            u32x4 o;
            o.x = pk2(gelu2(rs * __builtin_elementwise_fma(nm, lo2(c0), lo2(v0))));
            o.y = pk2(gelu2(rs * __builtin_elementwise_fma(nm, hi2(c0), hi2(v0))));
            o.z = pk2(gelu2(rs * __builtin_elementwise_fma(nm, lo2(c1), lo2(v1))));
            o.w = pk2(gelu2(rs * __builtin_elementwise_fma(nm, hi2(c1), hi2(v1))));
            st16<ST_AUX>(o, rO, lane * 16 + ((wi * 4 + m) * 28 + c * 4 + wj * 2 + h) * 1024);
          }
        }
      }
    } else {
      p1_loop<2>(lds, acc, w, lane, [&](int k) {
        if (k < 0) {
          tg = early_in();
          const float s = row4_sum(xr), s2 = __shfl_xor(s, 16);
          if (gid < 64 && (lane & 31) == 0) st16<ST_AUX>((u32x4){__float_as_uint(s), tg.a, __float_as_uint(s2), tg.b}, mk_rsrc(ws + WS_COL, 512 * 16), (row0 / 2 + (lane >> 5)) * 16);
        }
      });
      BAR();
      MEMFENCE();
      f32x2 z[4][2][4];
      {
        RowLN r;
        read_rowln(r, lds, wi, fq);
        f32x2 s1[2][4], s2[2][4];
#pragma unroll
        for (int n = 0; n < 4; ++n) {
          const float c1 = *(LAS float*)(lds + T_COL + (wj * 64 + n * 16 + fr) * 4);
          const f32x2 cc = {c1, c1};
#pragma unroll
          for (int h = 0; h < 2; ++h)
#pragma unroll
            for (int e = 0; e < 4; ++e) {
              const f32x2 g = gelu2(ln_val(r, acc, n, h, e, cc));
              z[n][h][e] = g;
              if (n == 0) { s1[h][e] = g; s2[h][e] = g * g; } else { s1[h][e] += g; s2[h][e] = __builtin_elementwise_fma(g, g, s2[h][e]); }
            }
        }
#pragma unroll
        for (int h = 0; h < 2; ++h) {
          const float v[16] = {s1[h][0].x, s2[h][0].x, s1[h][0].y, s2[h][0].y, s1[h][1].x, s2[h][1].x, s1[h][1].y, s2[h][1].y,
                               s1[h][2].x, s2[h][2].x, s1[h][2].y, s2[h][2].y, s1[h][3].x, s2[h][3].x, s1[h][3].y, s2[h][3].y};
          *(LAS float*)(lds + T_PART + ((wj * 128 + wi * 64 + 32 * h + 8 * fq) * 2 + fr) * 4) = reduce_scatter16(v, fr);
        }
      }
      asm volatile("s_waitcnt lgkmcnt(0)" ::: "memory");
      BAR();
      MEMFENCE();
      if (w < 2) {
        const int row = w * 64 + lane;
        const f32x2 p = *(LAS f32x2*)(lds + T_PART + row * 8) + *(LAS f32x2*)(lds + T_PART + (128 + row) * 8);
        st16<ST_AUX>((u32x4){__float_as_uint(p.x), tg.a, __float_as_uint(p.y), tg.b}, mk_rsrc(ws + WS_STAT + (size_t)pm * 7 * 2048, 7 * 2048), row * 16 + pn * 2048);
      }
      BAR();
      MEMFENCE();
      const rsrc_t rO = mk_rsrc(ws + WS_CT + (size_t)c * 8 * 65536 + pm * 4096, 8 * 65536);
#pragma unroll
      for (int h = 0; h < 2; ++h) {
        const int ro = T_FIN + (wi * 64 + 32 * h + 8 * fq) * 4;
        const f32x4 m0 = *(LAS f32x4*)(lds + ro), m1 = *(LAS f32x4*)(lds + ro + 16), r0 = *(LAS f32x4*)(lds + ro + 512), r1 = *(LAS f32x4*)(lds + ro + 528);
        const f32x2 nm[4] = {lo2(m0), hi2(m0), lo2(m1), hi2(m1)}, rs[4] = {lo2(r0), hi2(r0), lo2(r1), hi2(r1)};
#pragma unroll
        for (int n = 0; n < 4; ++n) {
          u32x4 o;
#pragma unroll
          for (int e = 0; e < 4; ++e) o[e] = pk2((z[n][h][e] + nm[e]) * rs[e]);
          st16<ST_AUX>(o, rO, lane * 16 + (wj * 4 + n) * 65536 + (wi * 2 + h) * 1024);
        }
      }
      publish_flag(ws, c, pm, w, lane, tg);
    }
    p2_loop(lds, acc, w, lane, [&](int it) {
      if (it == 0 && c < 7) publish_flag(ws, 15 + c, pm, w, lane, tg);
    });
    if (isY) {
      const rsrc_t rO = mk_rsrc(ws + WS_Y + ((size_t)type * 128 + pm * 8) * 16 * 1024, 8 * 16 * 1024);
#pragma unroll
      for (int n = 0; n < 4; ++n)
#pragma unroll
        for (int h = 0; h < 2; ++h) {
          const f32x4 v0 = acc[2 * h][n], v1 = acc[2 * h + 1][n];
          const u32x4 o = {pk2(lo2(v0)), pk2(hi2(v0)), pk2(lo2(v1)), pk2(hi2(v1))};
          st16<ST_AUX>(o, rO, lane * 16 + ((wj * 4 + n) * 16 + pn * 4 + wi * 2 + h) * 1024);
        }
    } else {
      const rsrc_t rO = mk_rsrc(ws + WS_H + (size_t)pm * 8 * 28 * 1024, 8 * 28 * 1024);
#pragma unroll
      for (int n = 0; n < 4; ++n) {
        const float b1 = bflo(bsr[n]);
        const f32x2 b = {b1, b1};
#pragma unroll
        for (int h = 0; h < 2; ++h) {
          const f32x4 v0 = acc[2 * h][n], v1 = acc[2 * h + 1][n];
          const u32x4 zz = *(LAS u32x4*)(lds + A_TAB + ((wj * 4 + n) * 4 + wi * 2 + h) * 1024 + lane * 16);
          u32x4 o;
          o.x = pk2(up2(zz.x) * (lo2(v0) + b));
          o.y = pk2(up2(zz.y) * (hi2(v0) + b));
          o.z = pk2(up2(zz.z) * (lo2(v1) + b));
          o.w = pk2(up2(zz.w) * (hi2(v1) + b));
          st16<ST_AUX>(o, rO, lane * 16 + ((wj * 4 + n) * 28 + pn * 4 + wi * 2 + h) * 1024);
        }
      }
    }
  }
  asm volatile("s_waitcnt lgkmcnt(0)" ::: "memory");
  BAR();
  MEMFENCE();
  if (!spare) publish_at(ws + WS_FL3 + (size_t)((pm * 2 + wj) * 32 + (isY ? 16 + c * 2 + wi : pn * 2 + wi)) * FS, lane, tg);
  {
    const unsigned lb = (unsigned)(size_t)lds;
    const int tt0 = pm * 8 + hb * 4;
    u32x2 xv[4];
#pragma unroll
    for (int n = 0; n < 4; ++n) xv[n] = *(const u32x2*)(x + (size_t)((tt0 + n) * 16 + fr) * D_ + on * 64 + w * 16 + 4 * fq);
    u32x2 wf[28][2];
    BAR();
    SCHED();
    {
      const int row = 4 * fq + (fr >> 2);
      const unsigned a0 = lb + row * 128 + ((w ^ ((row >> 1) & 3)) << 5) + (fr & 3) * 8, a1 = a0 + 57344;
      static_for<28>([&](auto S) {
        constexpr int s = S.value;
        dstr64<(s % 14) * 4096>(wf[s][0], s < 14 ? a0 : a1);
        dstr64<(s % 14) * 4096 + 2048>(wf[s][1], s < 14 ? a0 : a1);
        if constexpr ((s & 3) == 3) WAIT_L0();
      });
    }
    BAR();
    MEMFENCE();
    SCHED();
    u32x2 yr[4], yi[4];
    f32x4 oc[4];
#pragma unroll
    for (int n = 0; n < 4; ++n) oc[n] = (f32x4){0.f, 0.f, 0.f, 0.f};
    const unsigned h0 = lb + lane * 16, h1 = h0 + 65536;
    bf16x8 qa[8], qb[8];
    auto rdq = [&](bf16x8 (&q)[8], auto I, auto HC) {
      constexpr int i = I.value, hc = HC.value, ch = hc >> 1;
      ds128<(ch & 3) * 16384 + (i & 3) * 4096 + (2 * (hc & 1) + (i >> 2)) * 1024>(q[i], ch < 4 ? h0 : h1);
    };
    auto halfc = [&](const bf16x8 (&q)[8], bf16x8 (&g)[8], auto HC) {
      constexpr int hc = HC.value;
      static_for<8>([&](auto I) {
        constexpr int i = I.value, s = 2 * hc + (i >> 2);
        if constexpr (hc + 1 < 14) rdq(g, I, IC<hc + 1>{});
        asm volatile("s_waitcnt lgkmcnt(%0)" ::"n"(hc + 1 < 14 ? 8 : 7 - i) : "memory");
        SCHED();
        oc[i & 3] = MFMA(join(wf[s][0], wf[s][1]), q[i], oc[i & 3]);
        SCHED();
      });
    };
    SCHED();
    BAR();
    SCHED();
    static_for<8>([&](auto I) { rdq(qa, I, IC<0>{}); });
    static_for<7>([&](auto CH) {
      constexpr int ch = CH.value;
      halfc(qa, qb, IC<2 * ch>{});
      if constexpr (ch < 6) { BAR(); SCHED(); }
      if constexpr (ch == 3) {
        MEMFENCE();
        const rsrc_t rY = mk_rsrc(ws + WS_Y, 2 * 128 * 16 * 1024);
#pragma unroll
        for (int n = 0; n < 4; ++n) {
          yr[n] = __builtin_amdgcn_raw_buffer_load_b64(rY, lane * 16 + wj * 8, ((tt0 + n) * 16 + on * 2 + wi) * 1024, 16);
          yi[n] = __builtin_amdgcn_raw_buffer_load_b64(rY, lane * 16 + wj * 8, ((128 + tt0 + n) * 16 + on * 2 + wi) * 1024, 16);
        }
        SCHED();
      }
      halfc(qb, qa, IC<2 * ch + 1>{});
    });
#pragma unroll
    for (int n = 0; n < 4; ++n) {
      u32x2 o;
      o.x = pk2(up2(xv[n].x) + (up2(yr[n].x) - up2(yi[n].x)) + lo2(oc[n]));
      o.y = pk2(up2(xv[n].y) + (up2(yr[n].y) - up2(yi[n].y)) + hi2(oc[n]));
      __builtin_amdgcn_raw_buffer_store_b64(o, mk_rsrc(out, S_ * D_ * 2), (((tt0 + n) * 16 + fr) * D_ + on * 64 + w * 16 + 4 * fq) * 2, 0, 0);
    }
    asm volatile(".Lfm_e2:");
  }
#endif
}

extern "C" void kernel_launch(void* const* d_in, const int* in_sizes, int n_in, void* d_out, int out_size, void* d_ws,
                              size_t ws_size, hipStream_t stream) {
  const u16* x = (const u16*)d_in[0];
  const u16* ws_re = (const u16*)d_in[1];
  const u16* ws_im = (const u16*)d_in[2];
  const u16* wd_re = (const u16*)d_in[3];
  const u16* wd_im = (const u16*)d_in[4];
  const u16* w_in = (const u16*)d_in[6];
  const u16* w_sp = (const u16*)d_in[7];
  const u16* b_sp = (const u16*)d_in[8];
  const u16* w_out = (const u16*)d_in[9];
  (void)hipFuncSetAttribute((const void*)fm_main, hipFuncAttributeMaxDynamicSharedMemorySize, A_LDS);
  fm_main<<<256, 512, A_LDS, stream>>>(x, wd_re, wd_im, w_in, ws_re, ws_im, w_sp, b_sp, w_out, (char*)d_ws, (u16*)d_out);
}
```
